```python
import torch

def _softmax(x, axis=-1):
    return torch.softmax(x, dim=axis)

def _gelu(x):
    return torch.nn.functional.gelu(x, approximate='tanh')

def _silu(x):
    return torch.nn.functional.silu(x)

def _sigmoid(x):
    return torch.sigmoid(x)

def _layernorm(x, axis=-1, eps=1e-05):
    m = x.mean(dim=axis, keepdim=True)
    v = x.var(dim=axis, keepdim=True, unbiased=False)
    return (x - m) / torch.sqrt(v + eps)

def _scatter_add(tgt, idx, src, axis=0):
    return tgt.clone().index_add_(axis, idx, src)

def _segment_sum(src, seg, n):
    out = torch.zeros((n,) + src.shape[1:], dtype=src.dtype)
    return out.index_add_(0, seg, src)

def _pad(x, pw):
    flat = tuple((v for pair in reversed(pw) for v in pair))
    return torch.nn.functional.pad(x, flat)
kernel_name = 'adaLN_cond__spectral_gfnet__glu_gated_swiglu__prompt'

def setup_inputs():

    _w = lambda a: (lambda t: t.to(torch.bfloat16) if t.is_floating_point() else t)(torch.as_tensor(a))

    def _A_setup():
        B, S, D = (1, 2048, 512)
        Df = D // 2 + 1
        return {'x': _w((lambda *s: torch.randn(*s, dtype=torch.bfloat16) * 0.05)(B, S, D)), 'w_r': _w((lambda *s: torch.randn(*s, dtype=torch.bfloat16) * 1.6)(S, Df) * 0.02), 'w_i': _w((lambda *s: torch.randn(*s, dtype=torch.bfloat16) * 3.2)(S, Df) * 0.02)}
    _A_in = _A_setup()

    def _B_setup():
        B, S, D, F = (1, 2048, 512, 1792)
        return {'x': _w((lambda *s: torch.randn(*s, dtype=torch.bfloat16) * 0.05)(B, S, D)), 'w1': _w((lambda *s: torch.randn(*s, dtype=torch.bfloat16) * 0.4)(D, F) * (2.0 / D) ** 0.5), 'w3': _w((lambda *s: torch.randn(*s, dtype=torch.bfloat16) * 0.4)(D, F) * (2.0 / D) ** 0.5), 'w2': _w((lambda *s: torch.randn(*s, dtype=torch.bfloat16) * 0.4)(F, D) * (2.0 / F) ** 0.5)}
    _B_in = _B_setup()
    return {'A_x': _A_in['x'], 'A_w_r': _A_in['w_r'], 'A_w_i': _A_in['w_i'], 'B_x': _B_in['x'], 'B_w1': _B_in['w1'], 'B_w3': _B_in['w3'], 'B_w2': _B_in['w2'], 'c': _w((lambda *s: torch.randn(*s, dtype=torch.bfloat16) * 0.1)(1, 512) * 8.0), 'w_mod': _w((lambda *s: torch.randn(*s, dtype=torch.bfloat16) * 0.1)(512, 6 * 512) * 512 ** (-0.5) * 8.0)}

def reference(A_x, A_w_r, A_w_i, B_x, B_w1, B_w3, B_w2, c, w_mod):

    def _A_ref(x, w_r, w_i):
        B, S, D = x.shape
        X = (lambda x, axes=(-2, -1): torch.fft.rfft2(x.float(), dim=axes))(x, axes=(-2, -1))
        W = w_r + 1j * w_i
        X = X * W
        y = (lambda x, s, axes=(-2, -1): torch.fft.irfft2(x, s=s, dim=axes).to(torch.bfloat16))(X, (S, D), axes=(-2, -1))
        return y

    def _B_ref(x, w1, w3, w2):
        gate = torch.einsum('bsd,df->bsf', x, w1)
        up = torch.einsum('bsd,df->bsf', x, w3)
        h = _silu(gate) * up
        out = torch.einsum('bsf,fd->bsd', h, w2)
        return out
    x = A_x
    mod = (_silu(c) @ w_mod).reshape(c.shape[0], 6, 1, -1)
    sa, ca, ga, sb, cb, gb = (mod[:, 0].clone(), mod[:, 1].clone(), mod[:, 2].clone(), mod[:, 3].clone(), mod[:, 4].clone(), mod[:, 5].clone())
    h = _layernorm(x, axis=-1) * (1 + ca) + sa
    x = x + ga * _A_ref(h, A_w_r, A_w_i)
    h = _layernorm(x, axis=-1) * (1 + cb) + sb
    x = x + gb * _B_ref(h, B_w1, B_w3, B_w2)
    return x
```

```cpp
#include <hip/hip_runtime.h>
#include <stdint.h>

typedef unsigned u32;
typedef unsigned short u16;
typedef u32 u32x2 __attribute__((ext_vector_type(2)));
typedef u32 u32x4 __attribute__((ext_vector_type(4)));
typedef float f32x2 __attribute__((ext_vector_type(2)));
typedef float f32x4 __attribute__((ext_vector_type(4)));
typedef float f32x16 __attribute__((ext_vector_type(16)));
typedef __bf16 bf16x2 __attribute__((ext_vector_type(2)));
typedef __bf16 bf16x8 __attribute__((ext_vector_type(8)));
#define DI __device__ __forceinline__

constexpr size_t WS_MOD = 0;
constexpr size_t WS_U   = 65536;
constexpr size_t WS_U2  = WS_U + (2u << 20);
constexpr size_t WS_H2  = WS_U2 + (2u << 20);
constexpr size_t WS_X1  = WS_H2 + (2u << 20);
constexpr size_t WS_WT  = WS_X1 + (2u << 20);
constexpr size_t WS_W13 = WS_WT + (3u << 20);
constexpr size_t WS_W2  = WS_W13 + (4u << 20);
constexpr size_t WS_H   = WS_W2 + (2u << 20);

DI u32 pk(float lo, float hi) {
  f32x2 v = {lo, hi};
  return __builtin_bit_cast(u32, __builtin_convertvector(v, bf16x2));
}
DI float blo(u32 x) { return __builtin_bit_cast(float, x << 16); }
DI float bhi(u32 x) { return __builtin_bit_cast(float, x & 0xffff0000u); }
DI float rbf(float x) { return blo(pk(x, 0.f)); }
DI f32x4 mma16(u32x4 a, u32x4 b, f32x4 c) {
  return __builtin_amdgcn_mfma_f32_16x16x32_bf16(__builtin_bit_cast(bf16x8, a), __builtin_bit_cast(bf16x8, b), c, 0, 0, 0);
}
DI f32x16 mma32(u32x4 a, u32x4 b, f32x16 c) {
  return __builtin_amdgcn_mfma_f32_32x32x16_bf16(__builtin_bit_cast(bf16x8, a), __builtin_bit_cast(bf16x8, b), c, 0, 0, 0);
}
DI float cosr(float rev) { return __builtin_amdgcn_cosf(rev); }
DI float sinr(float rev) { return __builtin_amdgcn_sinf(rev); }
template <int CTRL, int RMASK = 0xf> DI float dpp_add(float v) {
  int t = __builtin_amdgcn_update_dpp(0, __builtin_bit_cast(int, v), CTRL, RMASK, 0xf, true);
  return v + __builtin_bit_cast(float, t);
}
DI float wave_sum(float v) {
  v = dpp_add<0xB1>(v);
  v = dpp_add<0x4E>(v);
  v = dpp_add<0x141>(v);
  v = dpp_add<0x140>(v);
  v = dpp_add<0x142, 0xa>(v);
  v = dpp_add<0x143, 0xc>(v);
  return __builtin_bit_cast(float, __builtin_amdgcn_readlane(__builtin_bit_cast(int, v), 63));
}
DI float silu(float g) { return g * __builtin_amdgcn_rcpf(1.f + __builtin_amdgcn_exp2f(-1.4426950408889634f * g)); }

DI int pos2idx(int p) { return ((p >> 2) & 15) + 16 * (4 * (p >> 6) + (p & 3)); }
DI int idx2pos(int m) { return 64 * (m >> 6) + 4 * (m & 15) + ((m >> 4) & 3); }

constexpr int NB_MOD = 192, NB_WT = 256, NB_W13 = 112, NB_W2 = 56;
__global__ __launch_bounds__(512) void k0(const u16* __restrict__ c, const u16* __restrict__ wmod, const u16* __restrict__ wr,
                                          const u16* __restrict__ wi, const u16* __restrict__ w1, const u16* __restrict__ w3,
                                          const u16* __restrict__ w2, unsigned char* __restrict__ ws) {
  __shared__ float part[32 * 16];
  const int t = threadIdx.x;
  int bid = blockIdx.x;
  if (bid < NB_MOD) {
    const int cj = (bid & 7) * 24 + (bid >> 3);
    const int row = t >> 1, half = t & 1;
    const u32x4 a0 = *(const u32x4*)(wmod + (size_t)row * 3072 + 16 * cj + 8 * half);
    const u32x4 a1 = *(const u32x4*)(wmod + (size_t)(row + 256) * 3072 + 16 * cj + 8 * half);
    const float s0 = rbf(silu(blo(c[row]))), s1 = rbf(silu(blo(c[row + 256])));
    float acc[8];
#pragma unroll
    for (int j = 0; j < 4; ++j) {
      acc[2 * j] = s0 * blo(a0[j]) + s1 * blo(a1[j]);
      acc[2 * j + 1] = s0 * bhi(a0[j]) + s1 * bhi(a1[j]);
    }
#pragma unroll
    for (int j = 0; j < 8; ++j) {
      float v = dpp_add<0x4E>(acc[j]);
      v = dpp_add<0x128>(v);
      acc[j] = dpp_add<0x124>(v);
    }
    if ((t & 15) < 2) {
      float* p = part + (t >> 4) * 16 + 8 * half;
      *(f32x4*)p = f32x4{acc[0], acc[1], acc[2], acc[3]};
      *(f32x4*)(p + 4) = f32x4{acc[4], acc[5], acc[6], acc[7]};
    }
    __syncthreads();
    if (t < 64) {
      const int col = t & 15, q = t >> 4;
      float s = 0.f;
#pragma unroll
      for (int j = 0; j < 8; ++j) s += part[(q * 8 + j) * 16 + col];
      s += __shfl_xor(s, 16);
      s += __shfl_xor(s, 32);
      if (t < 16) {
        const int idx = 16 * cj + col, vec = idx >> 9, d = idx & 511;
        float v = rbf(s);
        if (vec == 1 || vec == 4) v = 1.f + v;
        const int pos = vec < 2 ? idx2pos(d >> 1) * 2 + (d & 1) : d;
        ((u16*)(ws + WS_MOD))[vec * 512 + pos] = (u16)pk(v, 0.f);
      }
    }
    return;
  }
  bid -= NB_MOD;
  if (bid < NB_WT) {
    const int ka = bid & 7, g = (bid >> 3) & 3, fr = bid >> 5;
    u32x4* Wt = (u32x4*)(ws + WS_WT);
    {
      const int f = fr * 32 + (t & 31), r = t >> 5;
      u32x4 o;
#pragma unroll
      for (int j = 0; j < 4; ++j) {
        const int k = ka + 8 * r + 128 * (4 * g + j);
        o[j] = (u32)wr[k * 257 + f] | ((u32)wi[k * 257 + f] << 16);
      }
      Wt[(f * 8 + ka) * 64 + g * 16 + r] = o;
    }
    if (fr == 7 && t < 16) {
      u32x4 o;
#pragma unroll
      for (int j = 0; j < 4; ++j) {
        const int k = ka + 8 * t + 128 * (4 * g + j);
        o[j] = (u32)wr[k * 257 + 256] | ((u32)wi[k * 257 + 256] << 16);
      }
      Wt[(256 * 8 + ka) * 64 + g * 16 + t] = o;
    }
    return;
  }
  bid -= NB_WT;
  if (bid < NB_W13) {
    const int i = bid * 512 + t;
    const int kg = i / 896, fp = i % 896;
    const u32* p1 = (const u32*)w1 + (size_t)kg * 8 * 896 + fp;
    const u32* p3 = (const u32*)w3 + (size_t)kg * 8 * 896 + fp;
    u32 a[8], b[8];
#pragma unroll
    for (int j = 0; j < 8; ++j) { a[j] = p1[j * 896]; b[j] = p3[j * 896]; }
    u32x4 o0, o1, o2, o3;
#pragma unroll
    for (int j = 0; j < 4; ++j) {
      o0[j] = __builtin_amdgcn_perm(a[2 * j + 1], a[2 * j], 0x05040100);
      o1[j] = __builtin_amdgcn_perm(a[2 * j + 1], a[2 * j], 0x07060302);
      o2[j] = __builtin_amdgcn_perm(b[2 * j + 1], b[2 * j], 0x05040100);
      o3[j] = __builtin_amdgcn_perm(b[2 * j + 1], b[2 * j], 0x07060302);
    }
    u32x4* o = (u32x4*)(ws + WS_W13) + ((fp >> 2) * 16 + (kg >> 2)) * 64 + (kg & 3) * 16 + (fp & 3) * 4;
    o[0] = o0; o[1] = o1; o[2] = o2; o[3] = o3;
    return;
  }
  bid -= NB_W13;
  {
    const int i = bid * 512 + t;
    const int kg = i >> 7, nq = i & 127;
    const u32x2* p = (const u32x2*)w2 + (size_t)kg * 8 * 128 + nq;
    u32x2 a[8];
#pragma unroll
    for (int j = 0; j < 8; ++j) a[j] = p[j * 128];
    u32x4 o0, o1, o2, o3;
#pragma unroll
    for (int j = 0; j < 4; ++j) {
      o0[j] = __builtin_amdgcn_perm(a[2 * j + 1].x, a[2 * j].x, 0x05040100);
      o1[j] = __builtin_amdgcn_perm(a[2 * j + 1].x, a[2 * j].x, 0x07060302);
      o2[j] = __builtin_amdgcn_perm(a[2 * j + 1].y, a[2 * j].y, 0x05040100);
      o3[j] = __builtin_amdgcn_perm(a[2 * j + 1].y, a[2 * j].y, 0x07060302);
    }
    u32x4* o = (u32x4*)(ws + WS_W2) + ((nq >> 3) * 112 + (kg >> 1)) * 64 + (kg & 1) * 32 + (nq & 7) * 4;
    o[0] = o0; o[1] = o1; o[2] = o2; o[3] = o3;
  }
}

template <int SGN> DI void dft_frag(const int (&num)[4], float inv, u32x4& fre, u32x4& fim) {
#pragma unroll
  for (int j = 0; j < 4; ++j) {
    const float a = (float)num[j] * inv;
    const float cs = cosr(a), sn = SGN * sinr(a);
    fre[j] = pk(cs, sn);
    fim[j] = pk(-sn, cs);
  }
}
DI u32x4 twiddle_pack(f32x4 re, f32x4 im, const float (&c)[4], const float (&s)[4]) {
  u32x4 o;
#pragma unroll
  for (int j = 0; j < 4; ++j) o[j] = pk(re[j] * c[j] + im[j] * s[j], im[j] * c[j] - re[j] * s[j]);
  return o;
}
DI u32x4 pack4(f32x4 re, f32x4 im) {
  u32x4 o;
#pragma unroll
  for (int j = 0; j < 4; ++j) o[j] = pk(re[j], im[j]);
  return o;
}
constexpr int RP = 260;

__global__ __launch_bounds__(512) void k1(const u32* __restrict__ x, unsigned char* __restrict__ ws) {
  __shared__ __attribute__((aligned(16))) u32 R[8 * RP];
  const int t = threadIdx.x, w = t >> 6, lane = t & 63, r = lane & 15, g = lane >> 4;
  const int bp = (blockIdx.x & 7) * 32 + (blockIdx.x >> 3);
  const u32* xr = x + (size_t)(pos2idx(bp) + 256 * w) * 256 + r + 64 * g;
  u32 xv[4];
#pragma unroll
  for (int j = 0; j < 4; ++j) xv[j] = xr[16 * j];
  const u32x4 sa = ((const u32x4*)(ws + WS_MOD))[lane], c1 = ((const u32x4*)(ws + WS_MOD))[64 + lane];
  u32x4 fre, fim;
  float tc[4], ts[4];
  {
    int num[4];
#pragma unroll
    for (int j = 0; j < 4; ++j) num[j] = ((4 * g + j) * r) & 15;
    dft_frag<1>(num, 1.f / 16, fre, fim);
#pragma unroll
    for (int j = 0; j < 4; ++j) { const float a = (float)((4 * g + j) * r) * (1.f / 256); tc[j] = cosr(a); ts[j] = sinr(a); }
  }
  float v[8], s = 0.f;
#pragma unroll
  for (int j = 0; j < 4; ++j) { v[2 * j] = blo(xv[j]); v[2 * j + 1] = bhi(xv[j]); s += v[2 * j] + v[2 * j + 1]; }
  const float mean = wave_sum(s) * (1.f / 512);
  float q = 0.f;
#pragma unroll
  for (int j = 0; j < 8; ++j) { v[j] -= mean; q += v[j] * v[j]; }
  const float rstd = __builtin_amdgcn_rsqf(wave_sum(q) * (1.f / 512) + 1e-5f);
  u32x4 d;
#pragma unroll
  for (int j = 0; j < 4; ++j) d[j] = pk(v[2 * j] * rstd * blo(c1[j]) + blo(sa[j]), v[2 * j + 1] * rstd * bhi(c1[j]) + bhi(sa[j]));
  const f32x4 z = {0.f, 0.f, 0.f, 0.f};
  d = twiddle_pack(mma16(d, fre, z), mma16(d, fim, z), tc, ts);
  d = pack4(mma16(fre, d, z), mma16(fim, d, z));
  *(u32x4*)(R + w * RP + 4 * lane) = d;
  __syncthreads();
  const int p = t >> 1, h = t & 1;
  u32x4 o;
#pragma unroll
  for (int j = 0; j < 4; ++j) o[j] = R[(4 * h + j) * RP + p];
  ((u32x4*)(ws + WS_U))[(p * 256 + bp) * 2 + h] = o;
}

DI int wt_index(int f, int k) { return ((f * 8 + (k & 7)) * 64 + (k >> 9) * 16 + ((k >> 3) & 15)) * 4 + ((k >> 7) & 3); }
__global__ __launch_bounds__(512) void k2(unsigned char* __restrict__ ws) {
  __shared__ __attribute__((aligned(16))) u32 T[2 * 8 * 256];
  __shared__ __attribute__((aligned(16))) u32 T2[8 * 256];
  const int t = threadIdx.x, w = t >> 6, lane = t & 63, r = lane & 15, g = lane >> 4, r5 = lane & 31, h = lane >> 5;
  const int i = blockIdx.x >> 3, pr = (blockIdx.x & 7) * 16 + (i >> 1);
  const int m = (i & 1) ? (pr == 0 ? 128 : 256 - pr) : pr;
  const int mn = (256 - m) & 255;
  const u32x4* U = (const u32x4*)(ws + WS_U);
  const u32x4 u = U[(idx2pos(m) * 256 + 32 * w + r5) * 2 + h];
  const u32x4 v = U[(idx2pos(mn) * 256 + 32 * w + r5) * 2 + h];
  const u32* Wt = (const u32*)(ws + WS_WT);
  const u32x4 P4 = ((const u32x4*)Wt)[(m * 8 + w) * 64 + lane];
  u32 Q4[4], P4b[4];
  u32x4 Q4b;
#pragma unroll
  for (int j = 0; j < 4; ++j) Q4[j] = Wt[wt_index(256 - m, (2048 - (w + 8 * r + 128 * (4 * g + j))) & 2047)];
  if (m == 0) {
    Q4b = ((const u32x4*)Wt)[(256 * 8 + w) * 64 + lane];
#pragma unroll
    for (int j = 0; j < 4; ++j) P4b[j] = Wt[wt_index(0, (2048 - (w + 8 * r + 128 * (4 * g + j))) & 2047)];
  }
  u32x4 f8u, f8v, g8;
  {
    const int rho = lane & 15, reg = (rho & 3) + 4 * (rho >> 3), o = 4 * ((rho >> 2) & 1) + (reg >> 1), part = reg & 1;
#pragma unroll
    for (int j = 0; j < 4; ++j) {
      const float a = (float)((o * (4 * h + j)) & 7) * 0.125f;
      const float cs = cosr(a), sn = sinr(a);
      f8u[j] = part ? pk(-sn, cs) : pk(cs, sn);
      f8v[j] = f8u[j] ^ 0x80000000u;
      g8[j] = part ? pk(sn, cs) : pk(cs, -sn);
    }
  }
  u32x4 f1re, f1im, f2re, f2im, g1re, g1im, g2re, g2im;
  float tc[4], ts[4], uc[4], us[4];
  {
    int num[4];
#pragma unroll
    for (int j = 0; j < 4; ++j) num[j] = ((4 * g + j) * (w + 8 * r)) & 127;
    dft_frag<1>(num, 1.f / 128, f1re, f1im);
#pragma unroll
    for (int j = 0; j < 4; ++j) num[j] = ((4 * g + j) * r) & 15;
    dft_frag<1>(num, 1.f / 16, f2re, f2im);
    dft_frag<-1>(num, 1.f / 16, g1re, g1im);
#pragma unroll
    for (int j = 0; j < 4; ++j) num[j] = (r * (w + 8 * (4 * g + j))) & 127;
    dft_frag<-1>(num, 1.f / 128, g2re, g2im);
#pragma unroll
    for (int j = 0; j < 4; ++j) {
      const float a = (float)((4 * g + j) * (w + 8 * r)) * (1.f / 2048);
      tc[j] = cosr(a); ts[j] = sinr(a);
      const float b = (float)(r * (w + 8 * (4 * g + j))) * (1.f / 2048);
      uc[j] = cosr(b); us[j] = -sinr(b);
    }
  }
  const f32x16 z16 = {0.f};
  {
    const f32x16 du = mma32(f8u, u, z16), dv = mma32(f8v, v, z16);
#pragma unroll
    for (int j = 0; j < 4; ++j) {
      T[(4 * h + j) * 256 + 32 * w + r5] = pk(du[2 * j], du[2 * j + 1]);
      T[2048 + (4 * h + j) * 256 + 32 * w + r5] = pk(dv[2 * j], dv[2 * j + 1]);
    }
  }
  float Ar[4], Ai[4], Br[4], Bi[4];
  {
    const float sc = 0.5f / (2048.f * 256.f);
    const float th = (float)m * (1.f / 512), sn = sinr(th), cs = cosr(th);
#pragma unroll
    for (int j = 0; j < 4; ++j) {
      float pr_ = blo(P4[j]), pi_ = bhi(P4[j]), qr = blo(Q4[j]), qi = -bhi(Q4[j]);
      if (m == 0) {
        pr_ = 0.5f * (pr_ + blo(P4b[j])); pi_ = 0.5f * (pi_ - bhi(P4b[j]));
        qr = 0.5f * (qr + blo(Q4b[j])); qi = 0.5f * (qi + bhi(Q4b[j]));
      }
      const float dr = (pr_ - qr) * sc, di = (pi_ - qi) * sc;
      Ar[j] = (pr_ + qr) * sc - sn * dr;
      Ai[j] = (pi_ + qi) * sc - sn * di;
      Br[j] = -cs * di;
      Bi[j] = cs * dr;
    }
  }
  __syncthreads();
  u32x4 a = *(const u32x4*)(T + w * 256 + 4 * lane), b = *(const u32x4*)(T + 2048 + w * 256 + 4 * lane);
  const f32x4 z = {0.f, 0.f, 0.f, 0.f};
  a = twiddle_pack(mma16(a, f1re, z), mma16(a, f1im, z), tc, ts);
  b = twiddle_pack(mma16(b, f1re, z), mma16(b, f1im, z), tc, ts);
  {
    const f32x4 zr = mma16(f2re, a, z), zi = mma16(f2im, a, z), vr = mma16(f2re, b, z), vi = mma16(f2im, b, z);
#pragma unroll
    for (int j = 0; j < 4; ++j)
      a[j] = pk(Ar[j] * zr[j] - Ai[j] * zi[j] + Br[j] * vr[j] - Bi[j] * vi[j], Ar[j] * zi[j] + Ai[j] * zr[j] + Br[j] * vi[j] + Bi[j] * vr[j]);
  }
  a = twiddle_pack(mma16(a, g1re, z), mma16(a, g1im, z), uc, us);
  a = pack4(mma16(g2re, a, z), mma16(g2im, a, z));
  *(u32x4*)(T2 + w * 256 + 4 * lane) = a;
  __syncthreads();
#pragma unroll
  for (int j = 0; j < 4; ++j) a[j] = T2[(4 * h + j) * 256 + 32 * w + r5];
  const f32x16 dy = mma32(g8, a, z16);
#pragma unroll
  for (int j = 0; j < 4; ++j) a[j] = pk(dy[2 * j], dy[2 * j + 1]);
  ((u32x4*)(ws + WS_U2))[(idx2pos(m) * 256 + 32 * w + r5) * 2 + h] = a;
}

__global__ __launch_bounds__(512) void k3(const u32x4* __restrict__ x, unsigned char* __restrict__ ws) {
  __shared__ __attribute__((aligned(16))) u32 R[8 * RP];
  const int t = threadIdx.x, w = t >> 6, lane = t & 63, r = lane & 15, g = lane >> 4;
  const int bp = (blockIdx.x & 7) * 32 + (blockIdx.x >> 3);
  const int p = t >> 1, h = t & 1;
  const u32x4 u = ((const u32x4*)(ws + WS_U2))[(p * 256 + bp) * 2 + h];
  const int dq = 4 * r + g;
  const u32x4 xv = x[(size_t)(pos2idx(bp) + 256 * w) * 64 + dq];
  const u32x4* mod = (const u32x4*)(ws + WS_MOD);
  const u32x4 ga = mod[128 + dq], sb = mod[192 + dq], cb = mod[256 + dq];
  u32x4 fre, fim;
  float tc[4], ts[4];
  {
    int num[4];
#pragma unroll
    for (int j = 0; j < 4; ++j) num[j] = ((4 * g + j) * r) & 15;
    dft_frag<-1>(num, 1.f / 16, fre, fim);
#pragma unroll
    for (int j = 0; j < 4; ++j) { const float a = (float)((4 * g + j) * r) * (1.f / 256); tc[j] = cosr(a); ts[j] = -sinr(a); }
  }
#pragma unroll
  for (int j = 0; j < 4; ++j) R[(4 * h + j) * RP + p] = u[j];
  __syncthreads();
  u32x4 d = *(const u32x4*)(R + w * RP + 4 * lane);
  const f32x4 z = {0.f, 0.f, 0.f, 0.f};
  d = twiddle_pack(mma16(d, fre, z), mma16(d, fim, z), tc, ts);
  const f32x4 yr = mma16(d, fre, z), yi = mma16(d, fim, z);
  u32x4 x1;
#pragma unroll
  for (int j = 0; j < 4; ++j) x1[j] = pk(blo(xv[j]) + blo(ga[j]) * yr[j], bhi(xv[j]) + bhi(ga[j]) * yi[j]);
  ((u32x4*)(ws + WS_X1))[(size_t)(bp * 8 + w) * 64 + dq] = x1;
  float v[8], s = 0.f;
#pragma unroll
  for (int j = 0; j < 4; ++j) { v[2 * j] = blo(x1[j]); v[2 * j + 1] = bhi(x1[j]); s += v[2 * j] + v[2 * j + 1]; }
  const float mean = wave_sum(s) * (1.f / 512);
  float q = 0.f;
#pragma unroll
  for (int j = 0; j < 8; ++j) { v[j] -= mean; q += v[j] * v[j]; }
  const float rstd = __builtin_amdgcn_rsqf(wave_sum(q) * (1.f / 512) + 1e-5f);
#pragma unroll
  for (int j = 0; j < 4; ++j) d[j] = pk(v[2 * j] * rstd * blo(cb[j]) + blo(sb[j]), v[2 * j + 1] * rstd * bhi(cb[j]) + bhi(sb[j]));
  ((u32x4*)(ws + WS_H2))[((bp >> 1) * 16 + r) * 64 + g * 16 + (bp & 1) * 8 + w] = d;
}

DI void glds16(u32 lds_addr, u32 voff, const void* base) {
  asm volatile("s_mov_b32 m0, %0\n\ts_nop 0\n\tglobal_load_lds_dwordx4 %1, %2" ::"s"(lds_addr), "v"(voff), "s"(base) : "memory");
}
DI void gld16(u32x4& dst, u32 voff, const void* base) {
  asm volatile("global_load_dwordx4 %0, %1, %2" : "=v"(dst) : "v"(voff), "s"(base) : "memory");
}
template <int N> DI void wait_vm(u32x4& a, u32x4& b, u32x4& c, u32x4& d) {
  asm volatile("s_waitcnt vmcnt(%4)\n\ts_barrier" : "+v"(a), "+v"(b), "+v"(c), "+v"(d) : "i"(N) : "memory");
}
DI u32x4 transpose4(u32 a, u32 b, u32 c, u32 d) {
  auto s0 = __builtin_amdgcn_permlane16_swap(a, b, false, false);
  auto s1 = __builtin_amdgcn_permlane16_swap(c, d, false, false);
  auto s2 = __builtin_amdgcn_permlane32_swap(s0[0], s1[0], false, false);
  auto s3 = __builtin_amdgcn_permlane32_swap(s0[1], s1[1], false, false);
  return u32x4{s2[0], s3[0], s2[1], s3[1]};
}
DI u32 swiglu2(f32x4 a) { return pk(silu(a[0]) * a[2], silu(a[1]) * a[3]); }

template <bool FULL>
DI void k4_body(const unsigned char* ws, unsigned char* wsw, const u32x4* L, int rbk, int tau0, int tau1, int half, int w, int lane) {
  u32x4 W0[16], W1[16];
  const unsigned char* h2 = ws + WS_H2 + (size_t)(rbk * 8 + w) * 16384;
  const unsigned char* p0 = ws + WS_W13 + (size_t)tau0 * 16384;
  const unsigned char* p1 = ws + WS_W13 + (size_t)tau1 * 16384;
  const u32 voff = lane * 16;
  const u32 lbase = (u32)(size_t)(const __attribute__((address_space(3))) void*)L;
#pragma unroll
  for (int ks = 0; ks < 16; ++ks) {
    glds16(lbase + w * 16384 + ks * 1024, voff, h2 + ks * 1024);
    gld16(W0[ks], voff, p0 + ks * 1024);
    gld16(W1[ks], voff, p1 + ks * 1024);
  }
  f32x4 acc0[8], acc1[FULL ? 8 : 4];
#pragma unroll
  for (int i = 0; i < 8; ++i) acc0[i] = f32x4{0.f, 0.f, 0.f, 0.f};
#pragma unroll
  for (int i = 0; i < (FULL ? 8 : 4); ++i) acc1[i] = f32x4{0.f, 0.f, 0.f, 0.f};
  const u32x4* La = L + lane + (FULL ? 0 : half * 4096);
  const u32x4* Lb = L + lane + (FULL ? 4096 : (half ^ 1) * 4096);
#define K4_STAGE(S)                                                             \
  wait_vm<42 - 6 * (S)>(W0[2 * (S)], W0[2 * (S) + 1], W1[2 * (S)], W1[2 * (S) + 1]); \
  _Pragma("unroll") for (int kk = 0; kk < 2; ++kk) {                            \
    const int ks = 2 * (S) + kk;                                                \
    _Pragma("unroll") for (int i = 0; i < 4; ++i) {                             \
      const u32x4 hf = La[i * 1024 + ks * 64];                                  \
      acc0[i] = mma16(W0[ks], hf, acc0[i]);                                     \
      acc1[i] = mma16(W1[ks], hf, acc1[i]);                                     \
    }                                                                           \
    _Pragma("unroll") for (int i = 0; i < 4; ++i) {                             \
      const u32x4 hf = Lb[i * 1024 + ks * 64];                                  \
      acc0[4 + i] = mma16(W0[ks], hf, acc0[4 + i]);                             \
      if (FULL) acc1[(4 + i) & (FULL ? 7 : 3)] = mma16(W1[ks], hf, acc1[(4 + i) & (FULL ? 7 : 3)]); \
    }                                                                           \
  }
  K4_STAGE(0) K4_STAGE(1) K4_STAGE(2) K4_STAGE(3) K4_STAGE(4) K4_STAGE(5) K4_STAGE(6) K4_STAGE(7)
  u32x4* H = (u32x4*)(wsw + WS_H) + lane;
  const int mba = rbk * 2 + (FULL ? 0 : half), mbb = rbk * 2 + (FULL ? 1 : half ^ 1);
  H[(mba * 224 + tau0) * 64] = transpose4(swiglu2(acc0[0]), swiglu2(acc0[1]), swiglu2(acc0[2]), swiglu2(acc0[3]));
  H[(mba * 224 + tau1) * 64] = transpose4(swiglu2(acc1[0]), swiglu2(acc1[1]), swiglu2(acc1[2]), swiglu2(acc1[3]));
  H[(mbb * 224 + tau0) * 64] = transpose4(swiglu2(acc0[4]), swiglu2(acc0[5]), swiglu2(acc0[6]), swiglu2(acc0[7]));
  if (FULL) {
    constexpr int M = FULL ? 7 : 3;
    H[(mbb * 224 + tau1) * 64] = transpose4(swiglu2(acc1[4 & M]), swiglu2(acc1[5 & M]), swiglu2(acc1[6 & M]), swiglu2(acc1[7 & M]));
  }
}
__global__ __launch_bounds__(512, 2) void k4(unsigned char* __restrict__ ws) {
  __shared__ __attribute__((aligned(16))) u32x4 L[8192];
  const int lane = threadIdx.x & 63, w = __builtin_amdgcn_readfirstlane(threadIdx.x >> 6);
  const int xcd = blockIdx.x & 7, i = blockIdx.x >> 3;
  const int rbk = (xcd & 1) * 8 + (i & 7), fb = (xcd >> 1) * 4 + (i >> 3);
  if (w < 4) k4_body<true>(ws, ws, L, rbk, 14 * fb + 2 * w, 14 * fb + 2 * w + 1, 0, w, lane);
  else k4_body<false>(ws, ws, L, rbk, 14 * fb + 4 + w, 14 * fb + 12 + ((w - 4) >> 1), w & 1, w, lane);
}

constexpr int PP = 68;
__global__ __launch_bounds__(512, 2) void k5(const unsigned char* __restrict__ ws, u32x4* __restrict__ out) {
  __shared__ __attribute__((aligned(16))) float Pp[8 * 64 * PP];
  const int t = threadIdx.x, lane = t & 63, w = __builtin_amdgcn_readfirstlane(t >> 6), r5 = lane & 31, h = lane >> 5;
  const int xcd = blockIdx.x & 7, i = blockIdx.x >> 3;
  const int mb = xcd * 4 + (i & 3), nb = i >> 2;
  const u32x4* Hp = (const u32x4*)(ws + WS_H) + (mb * 224 + 28 * w + h) * 64 + r5;
  const u32x4* Wp = (const u32x4*)(ws + WS_W2) + (2 * nb * 112 + 14 * w) * 64 + lane;
  constexpr int WIN = 9;
  u32x4 fa[WIN][4];
#pragma unroll
  for (int j = 0; j < WIN; ++j) {
    fa[j][0] = Hp[j * 128]; fa[j][1] = Hp[j * 128 + 32];
    fa[j][2] = Wp[j * 64]; fa[j][3] = Wp[(112 + j) * 64];
  }
  const int row = t >> 3, c8 = t & 7;
  const u32x4 x1 = ((const u32x4*)(ws + WS_X1))[(size_t)(mb * 64 + row) * 64 + nb * 8 + c8];
  const u32x4 gb = ((const u32x4*)(ws + WS_MOD))[320 + nb * 8 + c8];
  __builtin_amdgcn_sched_barrier(0);
  f32x16 acc[2][2] = {};
#pragma unroll
  for (int j = 0; j < 14; ++j) {
    const u32x4 h0 = fa[j % WIN][0], h1 = fa[j % WIN][1], w0 = fa[j % WIN][2], w1 = fa[j % WIN][3];
    acc[0][0] = mma32(w0, h0, acc[0][0]);
    acc[0][1] = mma32(w1, h0, acc[0][1]);
    acc[1][0] = mma32(w0, h1, acc[1][0]);
    acc[1][1] = mma32(w1, h1, acc[1][1]);
    if (j + WIN < 14) {
      const int jn = j + WIN;
      fa[j % WIN][0] = Hp[jn * 128]; fa[j % WIN][1] = Hp[jn * 128 + 32];
      fa[j % WIN][2] = Wp[jn * 64]; fa[j % WIN][3] = Wp[(112 + jn) * 64];
    }
    __builtin_amdgcn_sched_barrier(0);
  }
#pragma unroll
  for (int a = 0; a < 2; ++a)
#pragma unroll
    for (int b = 0; b < 2; ++b)
#pragma unroll
      for (int q = 0; q < 4; ++q)
        *(f32x4*)(Pp + (w * 64 + a * 32 + r5) * PP + b * 32 + 8 * q + 4 * h) =
            f32x4{acc[a][b][4 * q], acc[a][b][4 * q + 1], acc[a][b][4 * q + 2], acc[a][b][4 * q + 3]};
  __syncthreads();
  f32x4 s0 = {0.f, 0.f, 0.f, 0.f}, s1 = s0;
#pragma unroll
  for (int k = 0; k < 8; ++k) {
    s0 += *(const f32x4*)(Pp + (k * 64 + row) * PP + 8 * c8);
    s1 += *(const f32x4*)(Pp + (k * 64 + row) * PP + 8 * c8 + 4);
  }
  u32x4 o;
  o[0] = pk(blo(x1[0]) + blo(gb[0]) * s0[0], bhi(x1[0]) + bhi(gb[0]) * s0[1]);
  o[1] = pk(blo(x1[1]) + blo(gb[1]) * s0[2], bhi(x1[1]) + bhi(gb[1]) * s0[3]);
  o[2] = pk(blo(x1[2]) + blo(gb[2]) * s1[0], bhi(x1[2]) + bhi(gb[2]) * s1[1]);
  o[3] = pk(blo(x1[3]) + blo(gb[3]) * s1[2], bhi(x1[3]) + bhi(gb[3]) * s1[3]);
  const int rho = mb * 64 + row;
  out[(size_t)(pos2idx(rho >> 3) + 256 * (rho & 7)) * 64 + nb * 8 + c8] = o;
}

#define EXTRA 0
__global__ void knop() {}

extern "C" void kernel_launch(void* const* d_in, const int* in_sizes, int n_in, void* d_out, int out_size, void* d_ws,
                              size_t ws_size, hipStream_t stream) {
  unsigned char* ws = (unsigned char*)d_ws;
  k0<<<NB_MOD + NB_WT + NB_W13 + NB_W2, 512, 0, stream>>>((const u16*)d_in[7], (const u16*)d_in[8], (const u16*)d_in[1],
                                                          (const u16*)d_in[2], (const u16*)d_in[4], (const u16*)d_in[5],
                                                          (const u16*)d_in[6], ws);
  k1<<<256, 512, 0, stream>>>((const u32*)d_in[0], ws);
  k2<<<256, 512, 0, stream>>>(ws);
  k3<<<256, 512, 0, stream>>>((const u32x4*)d_in[0], ws);
  k4<<<256, 512, 0, stream>>>(ws);
  k5<<<256, 512, 0, stream>>>(ws, (u32x4*)d_out);
  for (int i = 0; i < EXTRA; ++i) knop<<<256, 512, 0, stream>>>();
}
```

```cpp
#include <hip/hip_runtime.h>
typedef unsigned u32;

__global__ __launch_bounds__(256, 1) void fused(const void* x, const void* wr, const void* wi, const void* w1, const void* w3, const void* w2,
                                                const void* c, const void* wmod, void* ws, void* out) {
  __shared__ __attribute__((aligned(16))) unsigned char lds[163840];
  asm volatile(
      ".p2align 3\n"
      "s_load_dwordx16 s[36:51], s[58:59], 0x0\n"
      "s_load_dwordx4 s[52:55], s[58:59], 0x40\n"
      "s_getpc_b64 s[12:13]\n"
      ".Lq_Lpc:\n"
      "s_lshr_b32 s65, s56, 3\n"
      "v_and_b32 v130, 0xe0, v223\n"
      "s_mul_i32 s14, s65, 1024\n"
      "v_lshl_add_u32 v130, v130, 2, s14\n"
      "v_min_u32 v130, .Lq_Lcode_end-.Lq_Lpc-4, v130\n"
      "global_load_dword v130, v130, s[12:13] offset:0\n"
      "v_readfirstlane_b32 s57, v223\n"
      "v_lshlrev_b32 v231, 4, v223\n"
      "v_and_b32 v223, 63, v223\n"
      "s_and_b32 s15, s56, 7\n"
      "s_lshr_b32 s57, s57, 6\n"
      "v_lshrrev_b32 v0, 3, v223\n"
      "v_and_b32 v1, 6, v0\n"
      "v_and_b32 v2, 7, v223\n"
      "v_xor_b32 v1, v1, v2\n"
      "s_and_b32 s3, s65, 3\n"
      "s_lshl_b32 s1, s3, 2\n"
      "s_add_u32 s1, s1, s57\n"
      "s_mul_i32 s1, s1, 0x30000\n"
      "s_bfe_u32 s2, s65, 0x20002\n"
      "s_lshl_b32 s2, s2, 7\n"
      "s_add_u32 s1, s1, s2\n"
      "s_lshl_b32 s2, s15, 8\n"
      "s_add_u32 s1, s1, s2\n"
      "v_mul_u32_u24 v0, 0x1800, v0\n"
      "v_lshl_add_u32 v1, v1, 4, s1\n"
      "v_add_u32 v118, v0, v1\n"
      "s_add_u32 s2, s2, 2048\n"
      "v_add_u32 v120, s2, v118\n"
      "s_lshl_b32 s3, s3, 8\n"
      "v_lshl_add_u32 v128, v223, 2, s3\n"
      "s_lshl_b32 s68, s57, 12\n"
      "v_mov_b32 v129, 0\n"
      "s_and_b32 s1, s15, 1\n"
      "s_lshl_b32 s1, s1, 3\n"
      "s_and_b32 s2, s65, 7\n"
      "s_or_b32 s96, s1, s2\n"
      "s_lshr_b32 s1, s15, 1\n"
      "s_lshl_b32 s1, s1, 2\n"
      "s_lshr_b32 s2, s65, 3\n"
      "s_or_b32 s97, s1, s2\n"
      "s_lshl_b32 s1, s96, 4\n"
      "s_or_b32 s98, s1, s97\n"
      "s_bfe_u32 s1, s98, 0x40002\n"
      "s_and_b32 s2, s98, 0xc0\n"
      "s_or_b32 s1, s1, s2\n"
      "s_and_b32 s2, s98, 3\n"
      "s_lshl_b32 s2, s2, 4\n"
      "s_or_b32 s99, s1, s2\n"
      "s_lshl_b32 s1, s57, 19\n"
      "s_lshl_b32 s2, s99, 10\n"
      "s_add_u32 s1, s1, s2\n"
      "v_and_b32 v0, 15, v223\n"
      "v_and_b32 v1, 48, v223\n"
      "v_lshl_add_u32 v0, v0, 6, s1\n"
      "v_add_u32 v92, v1, v0\n"
      "v_add_u32 v93, 0x40000, v92\n"
      "s_lshl_b32 s1, s98, 13\n"
      "s_add_u32 s1, s1, 0x81000\n"
      "v_add_u32 v231, s1, v231\n"
      "v_accvgpr_write_b32 a252, -1\n"
      "v_accvgpr_write_b32 a253, -1\n"
      "v_accvgpr_write_b32 a254, -1\n"
      "v_accvgpr_write_b32 a255, -1\n"
      "s_lshr_b32 s1, s56, 6\n"
      "v_lshl_or_b32 v0, s57, 6, v223\n"
      "s_lshl_b32 s6, s56, 6\n"
      "v_lshlrev_b32_e32 v1, 4, v223\n"
      "s_xor_b32 s2, s1, 2\n"
      "s_lshl_b32 s5, s56, 1\n"
      "s_and_b32 s6, s6, 0x600\n"
      "v_lshrrev_b32_e32 v0, 2, v0\n"
      "v_and_b32_e32 v2, 0x180, v1\n"
      "s_and_b32 s0, s56, 7\n"
      "s_lshr_b32 s3, s2, 1\n"
      "s_and_b32 s5, s5, 64\n"
      "v_and_b32_e32 v0, 56, v0\n"
      "v_or_b32_e32 v2, s6, v2\n"
      "s_sub_i32 s4, 0, s3\n"
      "v_or3_b32 v0, v2, s5, v0\n"
      "v_mov_b32_e32 v2, s0\n"
      "v_bitop3_b32 v0, v0, s4, v2 bitop3:0x36\n"
      "v_and_b32_e32 v1, 0x70, v1\n"
      "v_add_u32_e32 v0, s3, v0\n"
      "v_bitop3_b32 v1, s1, v1, 2 bitop3:0xce\n"
      "v_and_b32_e32 v0, 0x7ff, v0\n"
      "v_lshl_or_b32 v1, s2, 7, v1\n"
      "s_movk_i32 s0, 0x202\n"
      "v_mad_u32_u24 v94, v0, s0, v1\n"
      "v_mov_b32_e32 v1, 0x100\n"
      "v_mad_u32_u24 v95, v0, s0, v1\n"
      "s_lshr_b32 s0, s56, 2\n"
      "s_and_b32 s0, s0, 0x3ffffffe\n"
      "s_lshr_b32 s1, s57, 1\n"
      "s_or_b32 s0, s0, s1\n"
      "s_mulk_i32 s0, 0xe00\n"
      "s_bfe_u32 s1, s56, 0x20001\n"
      "v_mov_b32_e32 v1, 0x380\n"
      "v_mov_b32_e32 v2, s0\n"
      "v_min_u32_e32 v0, 6, v223\n"
      "v_mad_u32_u24 v1, s1, v1, v2\n"
      "v_lshl_add_u32 v126, v0, 7, v1\n"
      "s_waitcnt lgkmcnt(0)\n"
      "global_load_dword v128, v128, s[48:49] offset:0\n"
      "s_cmp_ge_u32 s65, 8\n"
      "s_cbranch_scc1 .Lq_Lg0c\n"
      "s_add_u32 m0, s68, 0\n"
      "s_nop 0\n"
      "global_load_lds_dwordx4 v118, s[50:51]\n"
      "s_add_u32 m0, s68, 1024\n"
      "v_add_u32 v118, 0xc000, v118\n"
      "global_load_lds_dwordx4 v118, s[50:51]\n"
      "s_add_u32 m0, s68, 2048\n"
      "v_add_u32 v118, 0xc000, v118\n"
      "global_load_lds_dwordx4 v118, s[50:51]\n"
      "s_add_u32 m0, s68, 3072\n"
      "v_add_u32 v118, 0xc000, v118\n"
      "global_load_lds_dwordx4 v118, s[50:51]\n"
      ".Lq_Lg0c:\n"
      "global_load_dword v129, v129, s[52:53] offset:0 sc1\n"
      "s_mov_b32 s66, 0x38000\n"
      "s_bitcmp1_b32 s57, 0\n"
      "s_cselect_b64 s[70:71], s[44:45], s[42:43]\n"
      "s_add_u32 s90, s52, 0x201000\n"
      "s_addc_u32 s91, s53, 0\n"
      "s_add_u32 s92, s52, 0x402000\n"
      "s_addc_u32 s93, s53, 0\n"
      "global_load_dwordx4 v[84:87], v92, s[36:37] offset:0\n"
      "s_lshl_b32 s0, s57, 6\n"
      "v_lshrrev_b32_e32 v1, 4, v223\n"
      "v_or_b32_e32 v0, s0, v223\n"
      "v_and_or_b32 v3, v223, 48, s0\n"
      "v_lshlrev_b32_e32 v4, 9, v1\n"
      "v_lshlrev_b32_e32 v5, 5, v223\n"
      "s_movk_i32 s0, 0x180\n"
      "v_and_or_b32 v4, v5, s0, v4\n"
      "s_lshl_b32 s0, s57, 1\n"
      "v_lshrrev_b32_e32 v5, 1, v223\n"
      "v_and_or_b32 v5, v5, 1, s0\n"
      "s_lshl_b32 s0, s57, 8\n"
      "v_lshl_or_b32 v1, v1, 3, s0\n"
      "v_or_b32_e32 v116, 0x7800, v1\n"
      "v_mov_b32_e32 v1, 0x7800\n"
      "v_lshl_or_b32 v105, v0, 2, v1\n"
      "s_lshl_b32 s2, s56, 3\n"
      "v_lshlrev_b32_e32 v0, 14, v223\n"
      "s_lshl_b32 s0, s56, 9\n"
      "s_and_b32 s2, s2, 0x100\n"
      "v_and_b32_e32 v0, 0x1c000, v0\n"
      "s_and_b32 s1, s0, 0xe00\n"
      "v_or_b32_e32 v0, s2, v0\n"
      "s_and_b32 s0, s0, 0x3000\n"
      "v_or_b32_e32 v0, s1, v0\n"
      "v_or_b32_e32 v0, s0, v0\n"
      "s_mov_b32 s2, 0x40000\n"
      "v_or3_b32 v117, v0, v3, s2\n"
      "s_lshl_b32 s2, s56, 2\n"
      "v_lshlrev_b32_e32 v0, 2, v223\n"
      "s_and_b32 s2, s2, 0x180\n"
      "v_and_b32_e32 v0, 0x7c, v0\n"
      "v_or_b32_e32 v0, s2, v0\n"
      "v_lshrrev_b32_e32 v2, 2, v223\n"
      "v_or_b32_e32 v0, s1, v0\n"
      "v_bitop3_b32 v2, v2, v5, 6 bitop3:0x6c\n"
      "v_lshlrev_b32_e32 v5, 3, v223\n"
      "v_or_b32_e32 v0, s0, v0\n"
      "v_lshlrev_b32_e32 v2, 4, v2\n"
      "v_and_b32_e32 v5, 8, v5\n"
      "v_or_b32_e32 v119, 0x8000, v0\n"
      "v_lshrrev_b32_e32 v0, 1, v3\n"
      "v_or3_b32 v115, v2, v4, v5\n"
      "v_or_b32_e32 v159, 0x7c80, v0\n"
      "s_lshr_b32 s0, s56, 6\n"
      "s_xor_b32 s0, s0, 2\n"
      "s_lshr_b32 s1, s0, 1\n"
      "s_lshl_b32 s5, s56, 2\n"
      "s_sub_i32 s2, 0, s1\n"
      "v_and_b32_e32 v3, 7, v223\n"
      "global_load_dwordx4 v[88:91], v93, s[36:37] offset:0\n"
      "v_lshrrev_b32_e32 v4, 3, v223\n"
      "s_lshl_b32 s1, s1, 10\n"
      "s_and_b32 s5, s5, 0x80\n"
      "v_lshl_or_b32 v4, s57, 4, v4\n"
      "s_lshl_b32 s3, s56, 11\n"
      "v_lshlrev_b32_e32 v5, 4, v3\n"
      "s_or_b32 s1, s5, s1\n"
      "s_lshl_b32 s0, s0, 6\n"
      "v_mov_b32_e32 v8, s2\n"
      "s_and_b32 s3, s3, 0x3800\n"
      "s_lshl_b32 s4, s56, 5\n"
      "v_or_b32_e32 v6, s1, v5\n"
      "v_bitop3_b32 v8, s0, v8, v4 bitop3:0x36\n"
      "s_and_b32 s4, s4, 0x300\n"
      "v_or_b32_e32 v6, s3, v6\n"
      "v_or_b32_e32 v7, s0, v4\n"
      "v_lshlrev_b32_e32 v10, 2, v8\n"
      "v_or_b32_e32 v6, s4, v6\n"
      "v_bitop3_b32 v9, v7, 64, s2 bitop3:0x48\n"
      "v_and_b32_e32 v10, 60, v10\n"
      "v_bfe_u32 v8, v8, 4, 2\n"
      "v_add_u32_e32 v6, 0x884000, v6\n"
      "v_or3_b32 v8, v9, v10, v8\n"
      "v_lshl_add_u32 v96, v8, 14, v6\n"
      "v_or_b32_e32 v8, 8, v7\n"
      "v_bitop3_b32 v7, v7, s2, 8 bitop3:0x36\n"
      "v_lshlrev_b32_e32 v9, 2, v7\n"
      "v_bitop3_b32 v8, v8, 64, s2 bitop3:0x48\n"
      "v_and_b32_e32 v9, 60, v9\n"
      "v_bfe_u32 v7, v7, 4, 2\n"
      "v_or3_b32 v7, v8, v9, v7\n"
      "v_lshl_add_u32 v97, v7, 14, v6\n"
      "v_lshlrev_b32_e32 v6, 2, v223\n"
      "s_or_b32 s0, s5, s3\n"
      "v_and_b32_e32 v6, 0x7c, v6\n"
      "s_or_b32 s0, s0, s4\n"
      "v_lshl_or_b32 v0, s57, 6, v223\n"
      "v_or_b32_e32 v7, s0, v6\n"
      "v_or_b32_e32 v98, 0x4000, v7\n"
      "v_lshrrev_b32_e32 v7, 3, v0\n"
      "v_mov_b32_e32 v8, 0x7c00\n"
      "v_lshl_or_b32 v122, v7, 2, v8\n"
      "v_or_b32_e32 v123, 0x7c00, v6\n"
      "v_lshlrev_b32_e32 v6, 8, v7\n"
      "v_lshlrev_b32_e32 v7, 5, v3\n"
      "v_lshrrev_b32_e32 v0, 1, v0\n"
      "s_movk_i32 s0, 0x70\n"
      "v_and_b32_e32 v8, 0x70, v0\n"
      "v_bitop3_b32 v0, v0, v7, s0 bitop3:0x6c\n"
      "s_mov_b32 s0, 0x24010\n"
      "s_sub_u32 s12, s65, 8\n"
      "s_cmp_lt_u32 s12, 16\n"
      "s_cbranch_scc0 .Lq_Lg0n\n"
      "s_add_u32 m0, s68, 131072\n"
      "s_nop 0\n"
      "global_load_lds_dwordx4 v120, s[50:51]\n"
      "s_add_u32 m0, s68, 132096\n"
      "v_add_u32 v120, 0xc000, v120\n"
      "global_load_lds_dwordx4 v120, s[50:51]\n"
      "s_add_u32 m0, s68, 133120\n"
      "v_add_u32 v120, 0xc000, v120\n"
      "global_load_lds_dwordx4 v120, s[50:51]\n"
      "s_add_u32 m0, s68, 134144\n"
      "v_add_u32 v120, 0xc000, v120\n"
      "global_load_lds_dwordx4 v120, s[50:51]\n"
      ".Lq_Lg0n:\n"
      "v_bitop3_b32 v121, v0, s0, v6 bitop3:0x36\n"
      "v_lshlrev_b32_e32 v0, 10, v3\n"
      "v_lshlrev_b32_e32 v3, 2, v4\n"
      "v_xor_b32_e32 v4, v3, v5\n"
      "v_bitop3_b32 v3, v3, v0, v5 bitop3:0xde\n"
      "s_mov_b32 s0, 0x24020\n"
      "v_and_b32_e32 v1, 15, v223\n"
      "v_or_b32_e32 v100, 0x24000, v3\n"
      "v_bitop3_b32 v101, v4, s0, v0 bitop3:0x36\n"
      "s_mul_i32 s0, s57, 0x900\n"
      "v_and_b32_e32 v0, 48, v223\n"
      "v_lshlrev_b32_e32 v3, 3, v223\n"
      "v_lshrrev_b32_e32 v2, 4, v223\n"
      "s_addk_i32 s0, 0x5400\n"
      "v_lshl_or_b32 v0, v1, 6, v0\n"
      "v_and_b32_e32 v3, 0x60, v3\n"
      "s_movk_i32 s1, 0x48\n"
      "v_add3_u32 v102, v0, s0, v3\n"
      "v_mad_u32_u24 v0, v2, s1, v1\n"
      "v_bitop3_b32 v7, v8, v6, v7 bitop3:0xde\n"
      "v_lshl_add_u32 v103, v0, 2, s0\n"
      "v_or_b32_e32 v99, 0x24000, v7\n"
      "v_add_u32_e32 v104, 0x480, v103\n"
      "s_mul_i32 s0, s56, 7\n"
      "s_cmp_lt_u32 s56, 32\n"
      "v_lshrrev_b32_e32 v0, 4, v223\n"
      "s_cselect_b32 s0, s0, 0\n"
      "v_sub_u32_e64 v1, s56, 32 clamp\n"
      "v_or_b32_e32 v1, s0, v1\n"
      "v_lshlrev_b32_e32 v2, 10, v0\n"
      "v_lshl_or_b32 v1, v1, 13, v2\n"
      "v_lshlrev_b32_e32 v2, 4, v223\n"
      "s_lshl_b32 s0, s57, 8\n"
      "v_and_b32_e32 v2, 0xf0, v2\n"
      "v_or3_b32 v112, v1, s0, v2\n"
      "v_lshlrev_b32_e32 v1, 6, v223\n"
      "s_lshl_b32 s0, s57, 11\n"
      "v_and_b32_e32 v1, 0x300, v1\n"
      "v_lshlrev_b32_e32 v0, 5, v0\n"
      "v_or3_b32 v0, v1, s0, v0\n"
      "v_lshlrev_b32_e32 v1, 3, v223\n"
      "v_and_b32_e32 v1, 24, v1\n"
      "s_mov_b32 s0, 0x26000\n"
      "v_or_b32_e32 v113, 0x1000, v112\n"
      "v_or3_b32 v114, v0, v1, s0\n"
      "s_add_u32 s80, s52, 0x1000\n"
      "s_addc_u32 s81, s53, 0\n"
      "s_add_u32 s82, s52, 0x2000\n"
      "s_addc_u32 s83, s53, 0\n"
      "s_add_u32 s84, s52, 0x3000\n"
      "s_addc_u32 s85, s53, 0\n"
      "s_mov_b32 s69, 20000\n"
      "global_load_dwordx4 v[140:143], v94, s[38:39] offset:0\n"
      "s_mov_b32 s0, 0x8000\n"
      "v_lshrrev_b32_e32 v33, 2, v223\n"
      "v_and_b32_e32 v32, 15, v223\n"
      "v_and_b32_e32 v34, 12, v33\n"
      "v_mul_u32_u24_e32 v35, v34, v32\n"
      "v_mad_u32_u24 v34, v34, v32, v32\n"
      "v_cvt_f32_u32_e32 v36, v34\n"
      "v_add_u32_e32 v34, v34, v32\n"
      "v_or_b32_e32 v33, 3, v33\n"
      "v_cvt_f32_u32_e32 v34, v34\n"
      "v_mul_u32_u24_e32 v32, v33, v32\n"
      "v_cvt_f32_u32_e32 v32, v32\n"
      "v_cvt_f32_ubyte0_e32 v35, v35\n"
      "v_mul_f32_e32 v37, 0x3d800000, v35\n"
      "v_mul_f32_e32 v39, 0x3d800000, v36\n"
      "v_cos_f32_e32 v38, v37\n"
      "v_sin_f32_e32 v37, v37\n"
      "v_cos_f32_e32 v40, v39\n"
      "v_sin_f32_e32 v39, v39\n"
      "v_mul_f32_e32 v33, 0x3d800000, v34\n"
      "v_cos_f32_e32 v41, v33\n"
      "v_sin_f32_e32 v33, v33\n"
      "v_mul_f32_e32 v42, 0x3d800000, v32\n"
      "v_cos_f32_e32 v43, v42\n"
      "v_sin_f32_e32 v42, v42\n"
      "v_bfe_u32 v44, v223, 3, 1\n"
      "v_sub_u32_e64 v44, 0, v44\n"
      "v_cvt_pk_bf16_f32 v8, v38, v37\n"
      "v_cvt_pk_bf16_f32 v9, v40, v39\n"
      "v_cvt_pk_bf16_f32 v37, v37, v38\n"
      "v_cvt_pk_bf16_f32 v38, v39, v40\n"
      "v_mov_b32_e32 v40, 0x80008000\n"
      "v_cvt_pk_bf16_f32 v10, v41, v33\n"
      "v_cvt_pk_bf16_f32 v33, v33, v41\n"
      "v_bitop3_b32 v40, v44, s0, v40 bitop3:0x6c\n"
      "v_xor_b32_e32 v14, 0x8000, v33\n"
      "v_cvt_pk_bf16_f32 v39, v42, v43\n"
      "v_xor_b32_e64 v18, v40, v33\n"
      "v_mul_f32_e32 v33, 0x3b800000, v35\n"
      "v_mul_f32_e32 v35, 0x3b800000, v36\n"
      "v_mul_f32_e32 v34, 0x3b800000, v34\n"
      "v_mul_f32_e32 v32, 0x3b800000, v32\n"
      "v_cvt_pk_bf16_f32 v11, v43, v42\n"
      "global_load_dwordx4 v[144:147], v94, s[40:41] offset:0\n"
      "v_xor_b32_e32 v12, 0x8000, v37\n"
      "v_xor_b32_e32 v13, 0x8000, v38\n"
      "v_xor_b32_e32 v15, 0x8000, v39\n"
      "v_xor_b32_e32 v16, v40, v37\n"
      "v_xor_b32_e32 v17, v40, v38\n"
      "v_xor_b32_e32 v19, v40, v39\n"
      "v_cos_f32_e32 v20, v33\n"
      "v_cos_f32_e32 v21, v35\n"
      "v_cos_f32_e32 v22, v34\n"
      "v_cos_f32_e32 v23, v32\n"
      "v_sin_f32_e32 v24, v33\n"
      "v_sin_f32_e32 v25, v35\n"
      "v_sin_f32_e32 v26, v34\n"
      "v_sin_f32_e32 v27, v32\n"
      "s_lshl_b32 s0, s57, 5\n"
      "v_lshrrev_b32_e32 v33, 1, v223\n"
      "v_and_or_b32 v33, v33, 15, s0\n"
      "s_and_b32 s0, s0, 64\n"
      "v_lshlrev_b32_e32 v34, 3, v223\n"
      "v_and_or_b32 v35, v34, 48, s0\n"
      "v_lshrrev_b32_e32 v38, 2, v33\n"
      "v_and_or_b32 v38, v38, 11, v35\n"
      "v_sub_u32_e32 v38, 0, v38\n"
      "v_bfe_u32 v40, v38, 4, 2\n"
      "v_lshlrev_b32_e32 v39, 2, v38\n"
      "v_and_or_b32 v38, v38, 64, v40\n"
      "v_and_b32_e32 v37, 1, v223\n"
      "v_and_or_b32 v38, v39, 60, v38\n"
      "v_lshrrev_b32_e32 v32, 5, v223\n"
      "v_add_u32_e32 v38, 0x9c, v38\n"
      "v_cmp_eq_u32_e32 vcc, 0, v37\n"
      "v_mul_u32_u24_e32 v36, 0x1400, v32\n"
      "s_lshl_b32 s0, s98, 13\n"
      "v_cndmask_b32_e32 v37, v38, v33, vcc\n"
      "v_or_b32_e32 v33, 16, v33\n"
      "v_lshl_add_u32 v226, v37, 2, v36\n"
      "v_lshrrev_b32_e32 v37, 2, v33\n"
      "v_and_or_b32 v35, v37, 15, v35\n"
      "v_sub_u32_e32 v35, 0, v35\n"
      "v_bfe_u32 v38, v35, 4, 2\n"
      "v_lshlrev_b32_e32 v37, 2, v35\n"
      "v_and_or_b32 v35, v35, 64, v38\n"
      "v_and_or_b32 v35, v37, 60, v35\n"
      "s_cmp_lt_u32 s65, 24\n"
      "s_cbranch_scc1 .Lq_Le0\n"
      "global_load_ushort v156, v95, s[38:39] offset:0\n"
      "global_load_ushort v157, v95, s[40:41] offset:0\n"
      ".Lq_Le0:\n"
      "v_add_u32_e32 v35, 0x9c, v35\n"
      "v_cndmask_b32_e32 v33, v35, v33, vcc\n"
      "v_lshrrev_b32_e32 v38, 2, v223\n"
      "v_lshl_add_u32 v227, v33, 2, v36\n"
      "v_and_b32_e32 v36, 4, v223\n"
      "v_bfe_u32 v37, v223, 1, 1\n"
      "v_and_b32_e32 v38, 2, v38\n"
      "v_or3_b32 v36, v37, v36, v38\n"
      "v_lshlrev_b32_e32 v37, 2, v32\n"
      "v_lshlrev_b32_e32 v38, 1, v223\n"
      "v_mul_u32_u24_e32 v37, v36, v37\n"
      "v_and_b32_e32 v38, 2, v38\n"
      "v_or_b32_e32 v39, v37, v38\n"
      "v_add3_u32 v37, v37, v36, v38\n"
      "v_cvt_f32_u32_e32 v38, v37\n"
      "v_add_u32_e32 v37, v37, v36\n"
      "v_add_u32_e32 v36, v37, v36\n"
      "v_cvt_f32_u32_e32 v36, v36\n"
      "v_cvt_f32_u32_e32 v43, v37\n"
      "s_lshl_b32 s1, s57, 11\n"
      "s_add_i32 s0, s0, s1\n"
      "v_mul_f32_e32 v36, 0x3e000000, v36\n"
      "v_cos_f32_e32 v44, v36\n"
      "v_sin_f32_e32 v36, v36\n"
      "v_cvt_f32_u32_e32 v39, v39\n"
      "v_lshlrev_b32_e32 v40, 4, v223\n"
      "v_mul_u32_u24_e32 v32, 0x70, v32\n"
      "v_cvt_pk_bf16_f32 v31, v44, v36\n"
      "v_lshlrev_b32_e32 v36, 5, v223\n"
      "v_and_b32_e32 v36, 0x3c0, v36\n"
      "v_or_b32_e32 v36, s0, v36\n"
      "s_mul_i32 s0, s57, 0xa00\n"
      "v_lshl_or_b32 v33, s57, 6, v223\n"
      "v_add3_u32 v225, s0, v40, v32\n"
      "s_lshl_b32 s0, s56, 14\n"
      "v_lshlrev_b32_e32 v35, 1, v33\n"
      "v_mul_f32_e32 v38, 0x3e000000, v38\n"
      "v_mul_f32_e32 v37, 0x3e000000, v43\n"
      "s_and_b32 s0, s0, 0x1c000\n"
      "v_cos_f32_e32 v42, v38\n"
      "v_sin_f32_e32 v38, v38\n"
      "v_cos_f32_e32 v43, v37\n"
      "v_sin_f32_e32 v37, v37\n"
      "v_lshl_or_b32 v32, v33, 4, s0\n"
      "v_and_b32_e32 v33, 0xc0, v35\n"
      "v_and_b32_e32 v34, 56, v34\n"
      "v_bfe_u32 v35, v223, 3, 2\n"
      "v_mul_f32_e32 v39, 0x3e000000, v39\n"
      "s_lshl_b32 s0, s57, 10\n"
      "v_or3_b32 v33, v34, v33, v35\n"
      "v_cos_f32_e32 v41, v39\n"
      "v_sin_f32_e32 v39, v39\n"
      "v_lshl_or_b32 v33, v33, 2, s0\n"
      "v_or_b32_e32 v107, 0x4400, v33\n"
      "v_or_b32_e32 v108, 0x4410, v33\n"
      "v_lshlrev_b32_e32 v33, 14, v223\n"
      "v_cvt_pk_bf16_f32 v29, v42, v38\n"
      "v_cvt_pk_bf16_f32 v30, v43, v37\n"
      "v_and_b32_e32 v37, 32, v223\n"
      "v_and_b32_e32 v38, 16, v40\n"
      "v_and_b32_e32 v33, 0x1c000, v33\n"
      "v_or3_b32 v36, v36, v37, v38\n"
      "v_lshl_or_b32 v33, s56, 4, v33\n"
      "v_cvt_pk_bf16_f32 v28, v41, v39\n"
      "v_add_u32_e32 v224, 0x80000, v36\n"
      "v_or_b32_e32 v106, 0x40000, v32\n"
      "v_or_b32_e32 v109, 0x4400, v40\n"
      "v_or_b32_e32 v110, 0x20000, v33\n"
      "v_or_b32_e32 v127, 0x20000, v32\n"
      "s_waitcnt vmcnt(5)\n"
      "v_lshlrev_b32 v0, 16, v128\n"
      "v_and_b32 v1, 0xffff0000, v128\n"
      "v_mul_f32 v2, 0xbfb8aa3b, v0\n"
      "v_mul_f32 v3, 0xbfb8aa3b, v1\n"
      "v_exp_f32 v2, v2\n"
      "v_exp_f32 v3, v3\n"
      "v_add_f32 v2, 1.0, v2\n"
      "v_add_f32 v3, 1.0, v3\n"
      "v_rcp_f32 v2, v2\n"
      "v_rcp_f32 v3, v3\n"
      "v_mul_f32 v0, v0, v2\n"
      "v_mul_f32 v1, v1, v3\n"
      "v_cvt_pk_bf16_f32 v0, v0, v1\n"
      "ds_write_b32 v105, v0\n"
      "s_waitcnt lgkmcnt(0)\n"
      "s_cmp_ge_u32 s65, 8\n"
      "s_cbranch_scc1 .Lq_Lg1c\n"
      "ds_read2_b64 v[192:195], v116 offset0:0 offset1:4\n"
      "ds_read2_b64 v[196:199], v116 offset0:8 offset1:12\n"
      "ds_read2_b64 v[200:203], v116 offset0:16 offset1:20\n"
      "ds_read2_b64 v[204:207], v116 offset0:24 offset1:28\n"
      "s_waitcnt vmcnt(5)\n"
      "s_barrier\n"
      "ds_read_b64_tr_b16 v[176:177], v115 offset:0\n"
      "ds_read_b64_tr_b16 v[178:179], v115 offset:2048\n"
      "ds_read_b64_tr_b16 v[180:181], v115 offset:4096\n"
      "ds_read_b64_tr_b16 v[182:183], v115 offset:6144\n"
      "ds_read_b64_tr_b16 v[184:185], v115 offset:8192\n"
      "ds_read_b64_tr_b16 v[186:187], v115 offset:10240\n"
      "ds_read_b64_tr_b16 v[188:189], v115 offset:12288\n"
      "ds_read_b64_tr_b16 v[190:191], v115 offset:14336\n"
      "s_waitcnt lgkmcnt(6)\n"
      "v_mfma_f32_16x16x32_bf16 v[0:3], v[176:179], v[192:195], 0\n"
      "s_waitcnt lgkmcnt(4)\n"
      "v_mfma_f32_16x16x32_bf16 v[0:3], v[180:183], v[196:199], v[0:3]\n"
      "s_waitcnt lgkmcnt(2)\n"
      "v_mfma_f32_16x16x32_bf16 v[0:3], v[184:187], v[200:203], v[0:3]\n"
      "s_waitcnt lgkmcnt(0)\n"
      "v_mfma_f32_16x16x32_bf16 v[0:3], v[188:191], v[204:207], v[0:3]\n"
      "s_waitcnt vmcnt(4)\n"
      "v_add_u32 v5, 1, v129\n"
      "s_waitcnt vmcnt(4)\n"
      "v_add_u32 v7, 1, v129\n"
      "s_nop 5\n"
      "v_cvt_pk_bf16_f32 v4, v0, v1\n"
      "v_cvt_pk_bf16_f32 v6, v2, v3\n"
      "global_store_dwordx4 v117, v[4:7], s[52:53] offset:0 sc1\n"
      ".Lq_Lg1c:\n"
      "s_nop 1\n"
      "s_waitcnt vmcnt(4)\n"
      "v_add_u32 v214, 1, v129\n"
      "global_load_dword a240, v126, s[70:71] offset:0\n"
      "v_add_u32 v126, s66, v126\n"
      "s_lshl_b32 s63, s57, 13\n"
      "s_add_u32 s64, s63, 32768\n"
      "s_add_u32 s63, s63, 30720\n"
      "s_and_b32 s2, s97, 1\n"
      "s_mul_i32 s1, s97, 0xe0\n"
      "s_lshl_b32 s3, s2, 5\n"
      "s_sub_i32 s1, s1, s3\n"
      "s_lshr_b32 s3, s57, 1\n"
      "s_mul_i32 s4, s3, 0x1c000\n"
      "s_add_i32 s1, s1, s4\n"
      "v_and_b32_e32 v160, 15, v223\n"
      "v_lshrrev_b32_e32 v161, 4, v223\n"
      "s_movk_i32 s4, 0xe00\n"
      "v_mov_b32_e32 v162, s1\n"
      "v_mad_u32_u24 v162, v161, s4, v162\n"
      "v_lshlrev_b32_e32 v165, 11, v161\n"
      "v_lshlrev_b32_e32 v161, 1, v161\n"
      "v_bitop3_b32 v160, s57, v160, 1 bitop3:0x6c\n"
      "v_xor_b32_e32 v160, v160, v161\n"
      "global_load_dword a241, v126, s[70:71] offset:0\n"
      "v_add_u32 v126, s66, v126\n"
      "v_lshl_add_u32 v124, v160, 4, v162\n"
      "v_or_b32_e32 v160, 8, v161\n"
      "s_and_b32 s0, s57, 1\n"
      "v_bitop3_b32 v160, v223, v160, 15 bitop3:0x6c\n"
      "v_xor_b32_e32 v160, s0, v160\n"
      "v_lshlrev_b32_e32 v160, 4, v160\n"
      "s_movk_i32 s0, 0x7000\n"
      "s_lshl_b32 s1, s2, 1\n"
      "v_add3_u32 v125, v162, v160, s0\n"
      "s_add_i32 s2, s2, s57\n"
      "v_bfe_u32 v160, v223, 1, 1\n"
      "v_lshrrev_b32_e32 v163, 1, v223\n"
      "v_lshlrev_b32_e32 v166, 6, v223\n"
      "s_movk_i32 s4, 0x300\n"
      "v_lshl_or_b32 v161, s2, 1, v160\n"
      "v_and_or_b32 v165, v166, s4, v165\n"
      "v_bitop3_b32 v161, v163, v161, 14 bitop3:0x6c\n"
      "v_and_b32_e32 v162, 1, v223\n"
      "v_and_b32_e32 v164, 14, v163\n"
      "s_lshl_b32 s13, s57, 11\n"
      "s_add_u32 m0, s13, 155648\n"
      "s_nop 0\n"
      "global_load_lds_dwordx4 v112, s[46:47]\n"
      "s_add_u32 m0, s13, 156672\n"
      "s_nop 0\n"
      "global_load_lds_dwordx4 v113, s[46:47]\n"
      "v_or_b32_e32 v166, 0x8000, v165\n"
      "v_lshlrev_b32_e32 v161, 4, v161\n"
      "v_lshlrev_b32_e32 v163, 3, v162\n"
      "v_or3_b32 v236, v166, v163, v161\n"
      "v_xor_b32_e32 v161, 16, v161\n"
      "v_or_b32_e32 v163, v163, v165\n"
      "s_mov_b32 s0, 0xa000\n"
      "v_add3_u32 v237, v161, v163, s0\n"
      "s_add_i32 s0, s1, s57\n"
      "s_add_i32 s0, s0, 8\n"
      "v_bitop3_b32 v163, v164, s0, v162 bitop3:0x36\n"
      "s_or_b32 s0, s3, s1\n"
      "s_or_b32 s0, s0, 12\n"
      "v_lshl_or_b32 v161, v162, 13, v166\n"
      "v_bitop3_b32 v162, v164, s0, v162 bitop3:0x36\n"
      "v_lshlrev_b32_e32 v163, 4, v163\n"
      "v_lshlrev_b32_e32 v160, 3, v160\n"
      "v_lshlrev_b32_e32 v162, 4, v162\n"
      "v_or3_b32 v238, v163, v161, v160\n"
      "v_or3_b32 v239, v162, v161, v160\n"
      "global_load_dword a242, v126, s[70:71] offset:0\n"
      "v_add_u32 v126, s66, v126\n"
      "s_add_i32 s2, s98, 1\n"
      "s_and_b32 s1, s98, 0xfc\n"
      "s_and_b32 s2, s2, 3\n"
      "s_or_b32 s1, s2, s1\n"
      "s_lshr_b32 s2, s98, 1\n"
      "s_lshl_b32 s0, s57, 6\n"
      "s_bfe_u32 s3, s98, 0x40003\n"
      "s_lshl_b32 s4, s1, 3\n"
      "s_and_b32 s2, s2, 64\n"
      "v_or_b32_e32 v160, s0, v223\n"
      "s_and_b32 s4, s4, 48\n"
      "s_or_b32 s2, s2, s3\n"
      "s_or_b32 s2, s2, s4\n"
      "v_lshrrev_b32_e32 v160, 1, v160\n"
      "v_and_b32_e32 v168, 1, v223\n"
      "v_cvt_f32_ubyte0_e32 v165, s2\n"
      "v_lshlrev_b32_e32 v167, 13, v160\n"
      "s_lshl_b32 s2, s1, 5\n"
      "v_lshlrev_b32_e32 v169, 4, v168\n"
      "v_or3_b32 v167, v167, s2, v169\n"
      "s_movk_i32 s2, 0x410\n"
      "v_add_u32_e32 v228, 0x80000, v167\n"
      "v_mad_u32_u24 v160, v168, s2, v160\n"
      "v_mov_b32_e32 v167, 0x2780\n"
      "v_lshrrev_b32_e32 v162, 5, v223\n"
      "v_lshlrev_b32_e32 v163, 4, v223\n"
      "v_lshl_add_u32 v229, v160, 2, v167\n"
      "v_and_b32_e32 v167, 2, v223\n"
      "v_lshl_add_u32 v160, s57, 10, v163\n"
      "v_add3_u32 v167, v167, v162, -1\n"
      "v_add_u32_e32 v230, 0x2780, v160\n"
      "v_and_b32_e32 v160, 28, v223\n"
      "v_and_b32_e32 v167, 3, v167\n"
      "v_or3_b32 v160, v160, v167, s0\n"
      "v_and_b32_e32 v167, 16, v163\n"
      "v_lshlrev_b32_e32 v160, 13, v160\n"
      "v_lshl_or_b32 v167, s98, 5, v167\n"
      "s_mov_b32 s0, 0x281000\n"
      "v_add3_u32 v232, v167, v160, s0\n"
      "s_lshl_b32 s0, s98, 13\n"
      "s_lshl_b32 s2, s57, 11\n"
      "s_add_i32 s0, s0, s2\n"
      "v_mul_f32_e32 v165, 0x3b000000, v165\n"
      "v_add_u32_e32 v160, s0, v163\n"
      "s_lshl_b32 s0, s1, 13\n"
      "v_sin_f32_e32 v166, v165\n"
      "v_cos_f32_e32 v165, v165\n"
      "s_add_i32 s0, s0, s2\n"
      "v_add_u32_e32 v233, 0x482000, v160\n"
      "v_add_u32_e32 v160, s0, v163\n"
      "v_lshlrev_b32_e32 v164, 6, v223\n"
      "v_add_u32_e32 v234, 0x884000, v160\n"
      "v_not_b32_e32 v160, v223\n"
      "v_and_b32_e32 v161, 48, v223\n"
      "v_and_b32_e32 v164, 0x3c0, v164\n"
      "s_movk_i32 s0, 0x5800\n"
      "v_mov_b32_e32 v1, 0x35800000\n"
      "v_lshlrev_b32_e32 v160, 31, v160\n"
      "v_or3_b32 v235, v161, v164, s0\n"
      "v_fmamk_f32 v0, v166, 0xb5800000, v1\n"
      "v_fmac_f32_e32 v1, 0x35800000, v166\n"
      "v_mul_f32_e32 v2, 0x35800000, v165\n"
      "v_lshlrev_b32_e32 v3, 31, v162\n"
      "v_lshl_or_b32 v4, v168, 15, v160\n"
      "v_readfirstlane_b32 s72, v0\n"
      "v_readfirstlane_b32 s73, v1\n"
      "v_readfirstlane_b32 s74, v2\n"
      "s_add_u32 s86, s52, 0x100000\n"
      "s_addc_u32 s87, s53, 0\n"
      "s_add_u32 s88, s52, 0x40000\n"
      "s_addc_u32 s89, s53, 0\n"
      "s_mov_b32 s75, 0x36000000\n"
      "v_xor_b32 v48, 0x80000000, v8\n"
      "v_xor_b32 v52, 0x8000, v12\n"
      "v_xor_b32 v72, v4, v28\n"
      "v_xor_b32 v76, v3, v48\n"
      "global_load_dword a243, v126, s[70:71] offset:0\n"
      "v_add_u32 v126, s66, v126\n"
      "v_xor_b32 v80, v3, v52\n"
      "v_xor_b32 v49, 0x80000000, v9\n"
      "v_xor_b32 v53, 0x8000, v13\n"
      "v_xor_b32 v73, v4, v29\n"
      "v_xor_b32 v77, v3, v49\n"
      "v_xor_b32 v81, v3, v53\n"
      "v_xor_b32 v50, 0x80000000, v10\n"
      "v_xor_b32 v54, 0x8000, v14\n"
      "v_xor_b32 v74, v4, v30\n"
      "v_xor_b32 v78, v3, v50\n"
      "v_xor_b32 v82, v3, v54\n"
      "v_xor_b32 v51, 0x80000000, v11\n"
      "v_xor_b32 v55, 0x8000, v15\n"
      "v_xor_b32 v75, v4, v31\n"
      "v_xor_b32 v79, v3, v51\n"
      "v_xor_b32 v83, v3, v55\n"
      "s_mul_i32 s0, s56, 7\n"
      "s_cmp_lt_u32 s56, 32\n"
      "s_cselect_b32 s0, s0, 0\n"
      "v_sub_u32_e64 v162, s56, 32 clamp\n"
      "v_lshrrev_b32_e32 v160, 5, v223\n"
      "v_or_b32_e32 v162, s0, v162\n"
      "v_lshlrev_b32_e32 v161, 4, v223\n"
      "v_lshl_or_b32 v160, s57, 2, v160\n"
      "v_lshrrev_b32_e32 v163, 1, v162\n"
      "s_movk_i32 s0, 0x70\n"
      "v_lshlrev_b32_e32 v162, 5, v162\n"
      "v_mad_u32_u24 v160, v160, s0, v163\n"
      "v_and_b32_e32 v162, 32, v162\n"
      "v_bfe_u32 v163, v223, 1, 4\n"
      "v_and_b32_e32 v164, 16, v161\n"
      "v_or3_b32 v162, v163, v162, v164\n"
      "v_lshlrev_b32_e32 v162, 4, v162\n"
      "s_cmp_eq_u32 s57, 3\n"
      "v_lshl_or_b32 v160, v160, 10, v162\n"
      "s_cselect_b64 vcc, -1, 0\n"
      "s_lshl_b32 s0, s97, 7\n"
      "v_add_u32_e32 v6, 0xa85000, v160\n"
      "v_add_u32_e32 v7, 0xabd000, v160\n"
      "s_and_b32 s0, s0, 0x380\n"
      "v_and_b32_e32 v160, 0x70, v161\n"
      "v_or_b32_e32 v160, s0, v160\n"
      "v_cndmask_b32_e32 v160, v161, v160, vcc\n"
      "v_lshl_or_b32 v161, s57, 10, v160\n"
      "v_lshl_add_u32 v160, s57, 11, v160\n"
      "v_or_b32_e32 v208, 0x8000, v161\n"
      "v_add_u32_e32 v209, 0x5800, v160\n"
      "v_accvgpr_write_b32 a236, v6\n"
      "v_accvgpr_write_b32 a237, v7\n"
      "s_mul_i32 s61, s57, 8320\n"
      "s_add_u32 s61, s61, 30720\n"
      "s_mul_i32 s62, s57, 7168\n"
      "s_lshr_b32 s7, s98, 3\n"
      "s_xor_b32 s8, s7, 1\n"
      "s_lshl_b32 s9, s57, 13\n"
      "s_add_u32 s9, s9, 0x482000\n"
      "s_lshl_b32 s10, s7, 16\n"
      "s_add_u32 s10, s10, s9\n"
      "s_mul_i32 s11, s7, 229376\n"
      "s_add_u32 s11, s11, 0xc86000\n"
      "s_add_u32 s16, s52, s10\n"
      "s_addc_u32 s17, s53, 0\n"
      "s_add_u32 s18, s16, 32768\n"
      "s_addc_u32 s19, s17, 0\n"
      "s_add_u32 s24, s52, s11\n"
      "s_addc_u32 s25, s53, 0\n"
      "s_add_u32 s26, s24, 114688\n"
      "s_addc_u32 s27, s25, 0\n"
      "s_lshl_b32 s10, s8, 16\n"
      "s_add_u32 s10, s10, s9\n"
      "s_mul_i32 s11, s8, 229376\n"
      "s_add_u32 s11, s11, 0xc86000\n"
      "s_add_u32 s20, s52, s10\n"
      "s_addc_u32 s21, s53, 0\n"
      "s_add_u32 s22, s20, 32768\n"
      "s_addc_u32 s23, s21, 0\n"
      "s_add_u32 s28, s52, s11\n"
      "s_addc_u32 s29, s53, 0\n"
      "s_add_u32 s30, s28, 114688\n"
      "s_addc_u32 s31, s29, 0\n"
      "s_add_u32 s0, s98, 1\n"
      "s_and_b32 s0, s0, 2\n"
      "s_lshr_b32 s1, s98, 2\n"
      "s_or_b32 s60, s0, s1\n"
      "s_cbranch_scc1 .Lq_Lsp\n"
      "s_andn2_b32 s0, 1, s98\n"
      "s_lshl_b32 s1, s0, 2\n"
      "v_lshlrev_b32_e32 v160, 3, v223\n"
      "v_lshrrev_b32_e32 v161, 2, v223\n"
      "s_or_b32 s1, s1, s57\n"
      "v_and_b32_e32 v160, 0x78, v160\n"
      "v_and_b32_e32 v161, 12, v161\n"
      "v_or_b32_e32 v160, s1, v160\n"
      "v_mul_i32_i24_e32 v162, 0xffffff80, v161\n"
      "v_sub_u32_e32 v162, v162, v160\n"
      "v_lshlrev_b32_e32 v163, 11, v162\n"
      "v_lshrrev_b32_e32 v164, 1, v162\n"
      "v_lshlrev_b32_e32 v165, 1, v162\n"
      "v_and_b32_e32 v163, 0x3800, v163\n"
      "v_and_b32_e32 v164, 0x300, v164\n"
      "v_and_b32_e32 v165, 0xf0, v165\n"
      "v_lshrrev_b32_e32 v162, 5, v162\n"
      "v_lshl_or_b32 v160, v161, 7, v160\n"
      "v_and_b32_e32 v162, 12, v162\n"
      "v_or3_b32 v163, v164, v163, v165\n"
      "s_mov_b32 s2, 0x884000\n"
      "v_sub_u32_e32 v160, 0x680, v160\n"
      "v_or3_b32 v205, v163, v162, s2\n"
      "v_lshrrev_b32_e32 v161, 1, v160\n"
      "v_lshlrev_b32_e32 v162, 1, v160\n"
      "v_lshl_or_b32 v161, v160, 11, v161\n"
      "v_and_b32_e32 v162, 0xf0, v162\n"
      "v_lshrrev_b32_e32 v160, 5, v160\n"
      "s_movk_i32 s3, 0x3b00\n"
      "v_and_b32_e32 v160, 12, v160\n"
      "v_and_or_b32 v161, v161, s3, v162\n"
      "v_or3_b32 v206, v161, v160, s2\n"
      "s_lshl_b32 s2, s57, 6\n"
      "v_and_b32_e32 v160, 3, v223\n"
      "v_lshlrev_b32_e32 v161, 4, v223\n"
      "v_or_b32_e32 v162, s2, v223\n"
      "v_lshl_or_b32 v163, s1, 11, v161\n"
      "v_lshlrev_b32_e32 v162, 11, v162\n"
      "v_lshlrev_b32_e32 v160, 4, v160\n"
      "s_mov_b32 s1, 0x7e000\n"
      "v_and_b32_e32 v164, 1, v223\n"
      "v_and_or_b32 v160, v162, s1, v160\n"
      "v_bfe_u32 v162, v223, 1, 1\n"
      "v_mov_b32_e32 v165, 0xffffd880\n"
      "v_mov_b32_e32 v166, 0x2040\n"
      "v_cmp_eq_u32_e32 vcc, 0, v164\n"
      "v_or_b32_e32 v207, 0x4000, v163\n"
      "v_bitop3_b32 v163, v162, s98, 1 bitop3:0xd2\n"
      "v_cndmask_b32_e32 v165, v165, v166, vcc\n"
      "v_mul_u32_u24_e32 v164, 0x1040, v164\n"
      "v_cmp_eq_u32_e32 vcc, s0, v162\n"
      "v_or_b32_e32 v201, 0x80000, v160\n"
      "v_or_b32_e32 v202, 0x100000, v160\n"
      "v_bfrev_b32_e32 v160, 0.5\n"
      "v_sub_u32_e32 v163, 0, v163\n"
      "v_cndmask_b32_e32 v162, 0, v164, vcc\n"
      "v_bitop3_b32 v160, s2, v160, v223 bitop3:0xc8\n"
      "v_and_or_b32 v162, v165, v163, v162\n"
      "s_movk_i32 s0, 0x2780\n"
      "v_add3_u32 v203, v160, v162, s0\n"
      "s_lshl_b32 s0, s57, 10\n"
      "s_sub_i32 s0, 0x57c0, s0\n"
      "s_cmp_lg_u32 s57, 0\n"
      "s_cselect_b32 s0, s0, 0x2780\n"
      "v_add_u32_e32 v204, s0, v161\n"
      "v_accvgpr_write_b32 a224, v201\n"
      "v_accvgpr_write_b32 a225, v202\n"
      "v_accvgpr_write_b32 a226, v203\n"
      "v_accvgpr_write_b32 a227, v204\n"
      "v_accvgpr_write_b32 a228, v205\n"
      "v_accvgpr_write_b32 a229, v206\n"
      "v_accvgpr_write_b32 a230, v207\n"
      ".Lq_Lsp:\n"
      "s_waitcnt vmcnt(9)\n"
      "ds_write_b128 v102, v[84:87] offset:0\n"
      "s_waitcnt vmcnt(8)\n"
      "ds_write_b128 v102, v[88:91] offset:1152\n"
      "ds_read2_b32 v[132:133], v103 offset0:0 offset1:16\n"
      "ds_read2_b32 v[134:135], v103 offset0:32 offset1:48\n"
      "ds_read2_b32 v[136:137], v104 offset0:0 offset1:16\n"
      "ds_read2_b32 v[138:139], v104 offset0:32 offset1:48\n"
      "s_waitcnt lgkmcnt(0)\n"
      "s_sub_u32 s12, s65, 8\n"
      "s_cmp_lt_u32 s12, 16\n"
      "s_cbranch_scc0 .Lq_Lg1n\n"
      "ds_read2_b64 v[192:195], v116 offset0:0 offset1:4\n"
      "ds_read2_b64 v[196:199], v116 offset0:8 offset1:12\n"
      "ds_read2_b64 v[200:203], v116 offset0:16 offset1:20\n"
      "ds_read2_b64 v[204:207], v116 offset0:24 offset1:28\n"
      "v_add_u32 v115, 131072, v115\n"
      "s_waitcnt vmcnt(8)\n"
      "s_barrier\n"
      "ds_read_b64_tr_b16 v[160:161], v115 offset:0\n"
      "ds_read_b64_tr_b16 v[162:163], v115 offset:2048\n"
      "ds_read_b64_tr_b16 v[164:165], v115 offset:4096\n"
      "ds_read_b64_tr_b16 v[166:167], v115 offset:6144\n"
      "ds_read_b64_tr_b16 v[168:169], v115 offset:8192\n"
      "ds_read_b64_tr_b16 v[170:171], v115 offset:10240\n"
      "ds_read_b64_tr_b16 v[172:173], v115 offset:12288\n"
      "ds_read_b64_tr_b16 v[174:175], v115 offset:14336\n"
      "s_waitcnt lgkmcnt(6)\n"
      "v_mfma_f32_16x16x32_bf16 v[0:3], v[160:163], v[192:195], 0\n"
      "s_waitcnt lgkmcnt(4)\n"
      "v_mfma_f32_16x16x32_bf16 v[0:3], v[164:167], v[196:199], v[0:3]\n"
      "s_waitcnt lgkmcnt(2)\n"
      "v_mfma_f32_16x16x32_bf16 v[0:3], v[168:171], v[200:203], v[0:3]\n"
      "s_waitcnt lgkmcnt(0)\n"
      "v_mfma_f32_16x16x32_bf16 v[0:3], v[172:175], v[204:207], v[0:3]\n"
      "s_nop 7\n"
      "v_cvt_pk_bf16_f32 v4, v0, v1\n"
      "v_cvt_pk_bf16_f32 v5, v2, v3\n"
      "ds_write_b64 v159, v[4:5]\n"
      "s_waitcnt lgkmcnt(0)\n"
      ".Lq_Lg1n:\n"
      "s_nop 1\n"
      "s_mov_b32 s12, 0x05040100\n"
      "s_mov_b32 s13, 0x07060302\n"
      "s_waitcnt vmcnt(63) expcnt(7) lgkmcnt(15)\n"
      "s_waitcnt vmcnt(6)\n"
      "v_perm_b32 v148, v144, v140, s12\n"
      "v_perm_b32 v149, v144, v140, s13\n"
      "v_perm_b32 v150, v145, v141, s12\n"
      "v_perm_b32 v151, v145, v141, s13\n"
      "v_perm_b32 v152, v146, v142, s12\n"
      "v_perm_b32 v153, v146, v142, s13\n"
      "v_perm_b32 v154, v147, v143, s12\n"
      "v_perm_b32 v155, v147, v143, s13\n"
      "ds_write_b128 v99, v[148:151]\n"
      "ds_write_b128 v121, v[152:155]\n"
      "s_waitcnt vmcnt(6)\n"
      "v_lshl_or_b32 v156, v157, 16, v156\n"
      "ds_write_b32 v122, v156\n"
      "s_waitcnt lgkmcnt(0)\n"
      "s_barrier\n"
      "ds_read_b32 v156, v123\n"
      "ds_read_b32 v157, v123 offset:128\n"
      "ds_read2st64_b32 v[140:141], v100 offset0:0 offset1:1\n"
      "ds_read2st64_b32 v[142:143], v100 offset0:2 offset1:3\n"
      "ds_read2st64_b32 v[144:145], v101 offset0:0 offset1:1\n"
      "ds_read2st64_b32 v[146:147], v101 offset0:2 offset1:3\n"
      "s_mov_b32 s76, 0x3b000000\n"
      "s_waitcnt lgkmcnt(2)\n"
      "global_store_dwordx4 v96, v[140:143], s[52:53] offset:0 sc1\n"
      "s_mov_b32 s77, 0x3727c5ac\n"
      "v_lshlrev_b32 v176, 16, v132\n"
      "v_and_b32 v180, 0xffff0000, v132\n"
      "v_lshlrev_b32 v177, 16, v133\n"
      "v_and_b32 v181, 0xffff0000, v133\n"
      "v_lshlrev_b32 v178, 16, v134\n"
      "v_and_b32 v182, 0xffff0000, v134\n"
      "v_lshlrev_b32 v179, 16, v135\n"
      "v_and_b32 v183, 0xffff0000, v135\n"
      "v_pk_add_f32 v[4:5], v[176:177], v[178:179]\n"
      "v_pk_mul_f32 v[6:7], v[176:177], v[176:177]\n"
      "v_pk_add_f32 v[4:5], v[4:5], v[180:181]\n"
      "s_waitcnt lgkmcnt(0)\n"
      "global_store_dwordx4 v97, v[144:147], s[52:53] offset:0 sc1\n"
      "v_pk_fma_f32 v[6:7], v[178:179], v[178:179], v[6:7]\n"
      "v_pk_add_f32 v[4:5], v[4:5], v[182:183]\n"
      "v_pk_fma_f32 v[6:7], v[180:181], v[180:181], v[6:7]\n"
      "v_add_f32 v0, v4, v5\n"
      "v_pk_fma_f32 v[6:7], v[182:183], v[182:183], v[6:7]\n"
      "v_add_f32 v2, v6, v7\n"
      "v_lshlrev_b32 v184, 16, v136\n"
      "v_and_b32 v188, 0xffff0000, v136\n"
      "v_lshlrev_b32 v185, 16, v137\n"
      "v_and_b32 v189, 0xffff0000, v137\n"
      "v_lshlrev_b32 v186, 16, v138\n"
      "v_and_b32 v190, 0xffff0000, v138\n"
      "s_cmp_lt_u32 s65, 8\n"
      "s_cselect_b32 s12, 1, s57\n"
      "s_cmp_lg_u32 s12, 0\n"
      "s_cbranch_scc1 .Lq_Lt2\n"
      "s_cmp_lt_u32 s65, 24\n"
      "s_cselect_b64 vcc, -1, 0\n"
      "v_cndmask_b32 v156, v156, v157, vcc\n"
      "v_cndmask_b32 v98, v98, v119, vcc\n"
      "global_store_dword v98, v156, s[52:53] offset:0 sc1\n"
      ".Lq_Lt2:\n"
      "v_lshlrev_b32 v187, 16, v139\n"
      "v_and_b32 v191, 0xffff0000, v139\n"
      "v_pk_add_f32 v[4:5], v[184:185], v[186:187]\n"
      "v_pk_mul_f32 v[6:7], v[184:185], v[184:185]\n"
      "v_pk_add_f32 v[4:5], v[4:5], v[188:189]\n"
      "v_pk_fma_f32 v[6:7], v[186:187], v[186:187], v[6:7]\n"
      "v_pk_add_f32 v[4:5], v[4:5], v[190:191]\n"
      "v_pk_fma_f32 v[6:7], v[188:189], v[188:189], v[6:7]\n"
      "v_add_f32 v1, v4, v5\n"
      "v_pk_fma_f32 v[6:7], v[190:191], v[190:191], v[6:7]\n"
      "v_add_f32_e64 v3, v6, v7\n"
      "v_add_f32_dpp v0, v0, v0 quad_perm:[1,0,3,2] row_mask:0xf bank_mask:0xf\n"
      "global_store_dwordx4 v231, a[252:255], s[52:53] offset:-4096 sc1\n"
      "v_add_f32_dpp v2, v2, v2 quad_perm:[1,0,3,2] row_mask:0xf bank_mask:0xf\n"
      "v_add_f32_dpp v1, v1, v1 quad_perm:[1,0,3,2] row_mask:0xf bank_mask:0xf\n"
      "v_add_f32_dpp v3, v3, v3 quad_perm:[1,0,3,2] row_mask:0xf bank_mask:0xf\n"
      "v_add_f32_dpp v0, v0, v0 quad_perm:[2,3,0,1] row_mask:0xf bank_mask:0xf\n"
      "v_add_f32_dpp v2, v2, v2 quad_perm:[2,3,0,1] row_mask:0xf bank_mask:0xf\n"
      "v_add_f32_dpp v1, v1, v1 quad_perm:[2,3,0,1] row_mask:0xf bank_mask:0xf\n"
      "v_add_f32_dpp v3, v3, v3 quad_perm:[2,3,0,1] row_mask:0xf bank_mask:0xf\n"
      "v_add_f32_dpp v0, v0, v0 row_half_mirror row_mask:0xf bank_mask:0xf\n"
      "v_add_f32_dpp v2, v2, v2 row_half_mirror row_mask:0xf bank_mask:0xf\n"
      "v_add_f32_dpp v1, v1, v1 row_half_mirror row_mask:0xf bank_mask:0xf\n"
      "v_add_f32_dpp v3, v3, v3 row_half_mirror row_mask:0xf bank_mask:0xf\n"
      "v_add_f32_dpp v0, v0, v0 row_mirror row_mask:0xf bank_mask:0xf\n"
      "global_store_dwordx4 v231, a[252:255], s[52:53] offset:0 sc1\n"
      "v_add_f32_dpp v2, v2, v2 row_mirror row_mask:0xf bank_mask:0xf\n"
      "v_add_f32_dpp v1, v1, v1 row_mirror row_mask:0xf bank_mask:0xf\n"
      "v_add_f32_dpp v3, v3, v3 row_mirror row_mask:0xf bank_mask:0xf\n"
      "v_add_f32_dpp v0, v0, v0 row_bcast:15 row_mask:0xa bank_mask:0xf\n"
      "v_add_f32_dpp v2, v2, v2 row_bcast:15 row_mask:0xa bank_mask:0xf\n"
      "v_add_f32_dpp v1, v1, v1 row_bcast:15 row_mask:0xa bank_mask:0xf\n"
      "v_add_f32_dpp v3, v3, v3 row_bcast:15 row_mask:0xa bank_mask:0xf\n"
      "v_add_f32_dpp v0, v0, v0 row_bcast:31 row_mask:0xc bank_mask:0xf\n"
      "v_add_f32_dpp v2, v2, v2 row_bcast:31 row_mask:0xc bank_mask:0xf\n"
      "v_add_f32_dpp v1, v1, v1 row_bcast:31 row_mask:0xc bank_mask:0xf\n"
      "v_add_f32_dpp v3, v3, v3 row_bcast:31 row_mask:0xc bank_mask:0xf\n"
      "v_pk_mul_f32 v[0:1], v[0:1], s[76:77] op_sel:[0,0] op_sel_hi:[1,0]\n"
      "v_pk_mul_f32 v[2:3], v[2:3], s[76:77] op_sel:[0,0] op_sel_hi:[1,0]\n"
      "v_pk_fma_f32 v[2:3], v[0:1], v[0:1], v[2:3]  neg_lo:[1,0,0] neg_hi:[1,0,0]\n"
      "v_max_f32 v2, 0, v2\n"
      "v_max_f32 v3, 0, v3\n"
      "v_pk_add_f32 v[2:3], v[2:3], s[76:77] op_sel:[0,1] op_sel_hi:[1,1]\n"
      "v_rsq_f32 v2, v2\n"
      "v_rsq_f32 v3, v3\n"
      "s_nop 0\n"
      "v_pk_mul_f32 v[0:1], v[0:1], v[2:3] neg_lo:[1,0] neg_hi:[1,0]\n"
      "v_readlane_b32 s76, v2, 63\n"
      "v_readlane_b32 s78, v3, 63\n"
      "v_readlane_b32 s77, v0, 63\n"
      "v_readlane_b32 s79, v1, 63\n"
      "s_nop 0\n"
      "v_pk_fma_f32 v[176:177], v[176:177], s[76:77], s[76:77] op_sel:[0,0,1] op_sel_hi:[1,0,1]\n"
      "v_pk_fma_f32 v[178:179], v[178:179], s[76:77], s[76:77] op_sel:[0,0,1] op_sel_hi:[1,0,1]\n"
      "v_pk_fma_f32 v[180:181], v[180:181], s[76:77], s[76:77] op_sel:[0,0,1] op_sel_hi:[1,0,1]\n"
      "v_pk_fma_f32 v[182:183], v[182:183], s[76:77], s[76:77] op_sel:[0,0,1] op_sel_hi:[1,0,1]\n"
      "v_pk_fma_f32 v[184:185], v[184:185], s[78:79], s[78:79] op_sel:[0,0,1] op_sel_hi:[1,0,1]\n"
      "v_pk_fma_f32 v[186:187], v[186:187], s[78:79], s[78:79] op_sel:[0,0,1] op_sel_hi:[1,0,1]\n"
      "v_pk_fma_f32 v[188:189], v[188:189], s[78:79], s[78:79] op_sel:[0,0,1] op_sel_hi:[1,0,1]\n"
      "v_pk_fma_f32 v[190:191], v[190:191], s[78:79], s[78:79] op_sel:[0,0,1] op_sel_hi:[1,0,1]\n"
      "v_readfirstlane_b32 s67, v214\n"
      "s_cmp_ge_u32 s57, 2\n"
      "s_cselect_b32 s76, 1.0, 0\n"
      "s_cmp_eq_u32 s57, 2\n"
      "s_cselect_b32 s78, 1.0, 0\n"
      "global_load_dwordx4 v[192:195], v106, s[52:53] offset:0 sc1\n"
      "global_load_dwordx4 v[196:199], v106, s[80:81] offset:0 sc1\n"
      "global_load_dwordx4 v[200:203], v106, s[82:83] offset:0 sc1\n"
      "global_load_dwordx4 v[204:207], v106, s[84:85] offset:0 sc1\n"
      "s_branch .Lq_Lsk_m\n"
      ".Lq_Rm:\n"
      "s_sub_u32 s69, s69, 1\n"
      "s_cbranch_scc1 .Lq_Tm\n"
      "s_mov_b64 exec, vcc\n"
      "global_load_dwordx4 v[192:195], v106, s[52:53] offset:0 sc1\n"
      "global_load_dwordx4 v[196:199], v106, s[80:81] offset:0 sc1\n"
      "global_load_dwordx4 v[200:203], v106, s[82:83] offset:0 sc1\n"
      "global_load_dwordx4 v[204:207], v106, s[84:85] offset:0 sc1\n"
      "s_mov_b64 exec, -1\n"
      "s_waitcnt vmcnt(0)\n"
      "v_xor_b32 v0, s67, v193\n"
      "v_bitop3_b32 v0, v195, s67, v0 bitop3:0xbe\n"
      "v_bitop3_b32 v0, v197, s67, v0 bitop3:0xbe\n"
      "v_bitop3_b32 v0, v199, s67, v0 bitop3:0xbe\n"
      "v_bitop3_b32 v0, v201, s67, v0 bitop3:0xbe\n"
      "v_bitop3_b32 v0, v203, s67, v0 bitop3:0xbe\n"
      "v_bitop3_b32 v0, v205, s67, v0 bitop3:0xbe\n"
      "v_bitop3_b32 v0, v207, s67, v0 bitop3:0xbe\n"
      "v_cmp_ne_u32 vcc, 0, v0\n"
      "s_cbranch_vccnz .Lq_Rm\n"
      "s_branch .Lq_Cm\n"
      ".Lq_Tm:\n"
      "s_mov_b32 s69, 0\n"
      "s_branch .Lq_Cm\n"
      ".Lq_Lsk_m:\n"
      "global_store_dwordx4 v224, a[252:255], s[90:91] offset:0 sc1\n"
      "s_lshl_b32 s0, s98, 2\n"
      "v_lshlrev_b32_e32 v162, 3, v223\n"
      "v_and_b32_e32 v162, 0x78, v162\n"
      "v_lshrrev_b32_e32 v160, 2, v223\n"
      "v_and_or_b32 v162, s0, 4, v162\n"
      "v_and_b32_e32 v161, 12, v160\n"
      "v_bitop3_b32 v162, v162, 4, s57 bitop3:0x36\n"
      "v_mul_u32_u24_e32 v163, v161, v162\n"
      "v_cvt_f32_u32_e32 v163, v163\n"
      "v_mad_u32_u24 v161, v161, v162, v162\n"
      "v_cvt_f32_u32_e32 v164, v161\n"
      "v_add_u32_e32 v161, v161, v162\n"
      "v_or_b32_e32 v160, 3, v160\n"
      "v_cvt_f32_u32_e32 v161, v161\n"
      "v_mul_u32_u24_e32 v160, v160, v162\n"
      "v_cvt_f32_u32_e32 v160, v160\n"
      "v_mul_f32_e32 v162, 0x3c000000, v163\n"
      "v_cos_f32_e32 v165, v162\n"
      "global_store_dwordx4 v224, a[252:255], s[90:91] offset:1024 sc1\n"
      "v_sin_f32_e32 v162, v162\n"
      "v_mul_f32_e32 v166, 0x3c000000, v164\n"
      "v_cos_f32_e32 v167, v166\n"
      "v_sin_f32_e32 v166, v166\n"
      "v_mul_f32_e32 v168, 0x3c000000, v161\n"
      "v_cos_f32_e32 v169, v168\n"
      "v_sin_f32_e32 v168, v168\n"
      "v_mul_f32_e32 v170, 0x3c000000, v160\n"
      "v_cos_f32_e32 v171, v170\n"
      "v_sin_f32_e32 v170, v170\n"
      "v_cvt_pk_bf16_f32 v32, v165, v162\n"
      "global_load_dword a244, v126, s[70:71] offset:0\n"
      "v_add_u32 v126, s66, v126\n"
      "v_cvt_pk_bf16_f32 v162, v162, v165\n"
      "v_xor_b32_e32 v36, 0x8000, v162\n"
      "v_cvt_pk_bf16_f32 v162, v166, v167\n"
      "v_xor_b32_e32 v37, 0x8000, v162\n"
      "v_cvt_pk_bf16_f32 v162, v168, v169\n"
      "v_xor_b32_e32 v38, 0x8000, v162\n"
      "v_cvt_pk_bf16_f32 v162, v170, v171\n"
      "v_xor_b32_e32 v39, 0x8000, v162\n"
      "v_mul_f32_e32 v162, 0x3a000000, v163\n"
      "v_mul_f32_e32 v163, 0x3a000000, v164\n"
      "v_mul_f32_e32 v161, 0x3a000000, v161\n"
      "v_mul_f32_e32 v160, 0x3a000000, v160\n"
      "v_cvt_pk_bf16_f32 v33, v167, v166\n"
      "v_cvt_pk_bf16_f32 v34, v169, v168\n"
      "v_cvt_pk_bf16_f32 v35, v171, v170\n"
      "v_cos_f32_e32 v40, v162\n"
      "v_cos_f32_e32 v41, v163\n"
      "global_load_dword a245, v126, s[70:71] offset:0\n"
      "v_add_u32 v126, s66, v126\n"
      "v_cos_f32_e32 v42, v161\n"
      "v_cos_f32_e32 v43, v160\n"
      "v_sin_f32_e32 v44, v162\n"
      "v_sin_f32_e32 v45, v163\n"
      "v_sin_f32_e32 v46, v161\n"
      "v_sin_f32_e32 v47, v160\n"
      "s_lshl_b32 s0, s98, 2\n"
      "v_lshlrev_b32_e32 v161, 1, v223\n"
      "s_and_b32 s0, s0, 4\n"
      "v_and_b32_e32 v162, 0x60, v161\n"
      "v_or_b32_e32 v162, s0, v162\n"
      "v_and_b32_e32 v160, 15, v223\n"
      "global_load_dword a246, v126, s[70:71] offset:0\n"
      "v_add_u32 v126, s66, v126\n"
      "v_or_b32_e32 v163, s57, v162\n"
      "v_bitop3_b32 v162, v162, 4, s57 bitop3:0x36\n"
      "v_mul_u32_u24_e32 v162, v162, v160\n"
      "v_bitop3_b32 v164, v163, 4, 8 bitop3:0x36\n"
      "v_and_or_b32 v161, v161, -8, s0\n"
      "v_cvt_f32_u32_e32 v162, v162\n"
      "v_mul_u32_u24_e32 v164, v164, v160\n"
      "v_bitop3_b32 v163, v163, 4, 16 bitop3:0x36\n"
      "v_or_b32_e32 v161, s57, v161\n"
      "v_cvt_f32_u32_e32 v164, v164\n"
      "v_mul_u32_u24_e32 v163, v163, v160\n"
      "v_bitop3_b32 v161, v161, 4, 24 bitop3:0x36\n"
      "v_cvt_f32_u32_e32 v163, v163\n"
      "v_mul_lo_u32 v160, v161, v160\n"
      "v_cvt_f32_u32_e32 v160, v160\n"
      "v_mul_f32_e32 v161, 0x3c000000, v162\n"
      "v_cos_f32_e32 v165, v161\n"
      "global_load_dword a247, v126, s[70:71] offset:0\n"
      "v_add_u32 v126, s66, v126\n"
      "v_sin_f32_e32 v161, v161\n"
      "v_mul_f32_e32 v166, 0x3c000000, v164\n"
      "v_cos_f32_e32 v167, v166\n"
      "v_sin_f32_e32 v166, v166\n"
      "v_mul_f32_e32 v169, 0x3c000000, v163\n"
      "v_cos_f32_e32 v170, v169\n"
      "v_sin_f32_e32 v169, v169\n"
      "v_mul_f32_e32 v171, 0x3c000000, v160\n"
      "v_cos_f32_e32 v172, v171\n"
      "v_sin_f32_e64 v171, v171\n"
      "v_cvt_pk_bf16_f32 v168, v165, v161\n"
      "v_xor_b32_e32 v64, 0x80000000, v168\n"
      "v_cvt_pk_bf16_f32 v168, v167, v166\n"
      "v_xor_b32_e32 v65, 0x80000000, v168\n"
      "v_cvt_pk_bf16_f32 v168, v170, v169\n"
      "v_xor_b32_e32 v66, 0x80000000, v168\n"
      "v_cvt_pk_bf16_f32 v168, v172, v171\n"
      "v_cvt_pk_bf16_f32 v68, v161, v165\n"
      "v_mul_f32_e32 v161, 0x3a000000, v162\n"
      "v_mul_f32_e32 v162, 0x3a000000, v164\n"
      "v_mul_f32_e32 v163, 0x3a000000, v163\n"
      "v_mul_f32_e32 v160, 0x3a000000, v160\n"
      "v_xor_b32_e32 v67, 0x80000000, v168\n"
      "v_cvt_pk_bf16_f32 v69, v166, v167\n"
      "v_cvt_pk_bf16_f32 v70, v169, v170\n"
      "v_cvt_pk_bf16_f32 v71, v171, v172\n"
      "v_cos_f32_e32 v56, v161\n"
      "v_cos_f32_e32 v57, v162\n"
      "v_cos_f32_e32 v58, v163\n"
      "v_cos_f32_e32 v59, v160\n"
      "v_sin_f32_e32 v60, v161\n"
      "v_sin_f32_e32 v61, v162\n"
      "v_sin_f32_e32 v62, v163\n"
      "v_sin_f32_e32 v63, v160\n"
      "s_waitcnt vmcnt(9)\n"
      "v_xor_b32 v0, s67, v193\n"
      "v_bitop3_b32 v0, v195, s67, v0 bitop3:0xbe\n"
      "s_waitcnt vmcnt(8)\n"
      "v_bitop3_b32 v0, v197, s67, v0 bitop3:0xbe\n"
      "v_bitop3_b32 v0, v199, s67, v0 bitop3:0xbe\n"
      "s_waitcnt vmcnt(7)\n"
      "v_bitop3_b32 v0, v201, s67, v0 bitop3:0xbe\n"
      "v_bitop3_b32 v0, v203, s67, v0 bitop3:0xbe\n"
      "s_waitcnt vmcnt(6)\n"
      "v_bitop3_b32 v0, v205, s67, v0 bitop3:0xbe\n"
      "v_bitop3_b32 v0, v207, s67, v0 bitop3:0xbe\n"
      "v_cmp_ne_u32 vcc, 0, v0\n"
      "s_cbranch_vccnz .Lq_Rm\n"
      ".Lq_Cm:\n"
      "v_lshlrev_b32 v160, 16, v192\n"
      "v_and_b32 v161, 0xffff0000, v192\n"
      "v_lshlrev_b32 v162, 16, v194\n"
      "v_and_b32 v163, 0xffff0000, v194\n"
      "v_pk_add_f32 v[160:161], v[160:161], s[76:77] op_sel_hi:[1,0]\n"
      "v_pk_add_f32 v[162:163], v[162:163], s[76:77] op_sel_hi:[1,0]\n"
      "v_lshlrev_b32 v164, 16, v196\n"
      "v_and_b32 v165, 0xffff0000, v196\n"
      "v_lshlrev_b32 v166, 16, v198\n"
      "v_and_b32 v167, 0xffff0000, v198\n"
      "v_pk_add_f32 v[160:161], v[160:161], v[164:165]\n"
      "v_pk_add_f32 v[162:163], v[162:163], v[166:167]\n"
      "v_lshlrev_b32 v164, 16, v200\n"
      "v_and_b32 v165, 0xffff0000, v200\n"
      "v_lshlrev_b32 v166, 16, v202\n"
      "v_and_b32 v167, 0xffff0000, v202\n"
      "v_pk_add_f32 v[160:161], v[160:161], v[164:165]\n"
      "v_pk_add_f32 v[162:163], v[162:163], v[166:167]\n"
      "v_lshlrev_b32 v164, 16, v204\n"
      "v_and_b32 v165, 0xffff0000, v204\n"
      "v_lshlrev_b32 v166, 16, v206\n"
      "v_and_b32 v167, 0xffff0000, v206\n"
      "v_pk_add_f32 v[160:161], v[160:161], v[164:165]\n"
      "v_pk_add_f32 v[162:163], v[162:163], v[166:167]\n"
      "ds_write2st64_b32 v107, v160, v161 offset0:0 offset1:4\n"
      "ds_write2st64_b32 v108, v162, v163 offset0:0 offset1:4\n"
      "s_waitcnt lgkmcnt(0)\n"
      "s_barrier\n"
      "ds_read_b128 v[168:171], v109 offset:2048\n"
      "ds_read_b128 v[160:163], v109 offset:0\n"
      "ds_read_b128 v[172:175], v109 offset:3072\n"
      "ds_read_b128 v[164:167], v109 offset:1024\n"
      "s_cmp_lg_u32 s57, 0\n"
      "s_cbranch_scc1 .Lq_Lf0\n"
      "global_store_dword v110, v214, s[52:53] offset:0 sc1\n"
      ".Lq_Lf0:\n"
      "s_add_u32 m0, s64, 0\n"
      "s_nop 0\n"
      "global_load_lds_dwordx4 v124, s[70:71]\n"
      "s_waitcnt lgkmcnt(2)\n"
      "v_pk_fma_f32 v[176:177], v[176:177], v[168:169], v[160:161]\n"
      "v_pk_fma_f32 v[178:179], v[178:179], v[170:171], v[162:163]\n"
      "s_waitcnt vmcnt(63) expcnt(7) lgkmcnt(15)\n"
      "s_waitcnt lgkmcnt(0)\n"
      "v_pk_fma_f32 v[180:181], v[180:181], v[172:173], v[164:165]\n"
      "v_pk_fma_f32 v[182:183], v[182:183], v[174:175], v[166:167]\n"
      "v_pk_fma_f32 v[184:185], v[184:185], v[168:169], v[160:161]\n"
      "v_pk_fma_f32 v[186:187], v[186:187], v[170:171], v[162:163]\n"
      "v_pk_fma_f32 v[188:189], v[188:189], v[172:173], v[164:165]\n"
      "v_pk_fma_f32 v[190:191], v[190:191], v[174:175], v[166:167]\n"
      "v_cvt_pk_bf16_f32 v152, v176, v180\n"
      "v_cvt_pk_bf16_f32 v153, v177, v181\n"
      "v_cvt_pk_bf16_f32 v154, v178, v182\n"
      "v_cvt_pk_bf16_f32 v155, v179, v183\n"
      "v_cvt_pk_bf16_f32 v156, v184, v188\n"
      "v_cvt_pk_bf16_f32 v157, v185, v189\n"
      "v_mfma_f32_16x16x32_bf16 v[128:131], v[152:155], v[8:11], 0\n"
      "v_cvt_pk_bf16_f32 v158, v186, v190\n"
      "v_cvt_pk_bf16_f32 v159, v187, v191\n"
      "v_mfma_f32_16x16x32_bf16 v[132:135], v[152:155], v[12:15], 0\n"
      "s_nop 0\n"
      "v_mfma_f32_16x16x32_bf16 v[136:139], v[156:159], v[8:11], 0\n"
      "v_mfma_f32_16x16x32_bf16 v[140:143], v[156:159], v[12:15], 0\n"
      "s_add_u32 m0, s64, 1024\n"
      "v_add_u32 v124, 0x3800, v124\n"
      "global_load_lds_dwordx4 v124, s[70:71]\n"
      "v_pk_mul_f32 v[144:145], v[128:129], v[20:21]\n"
      "s_nop 0\n"
      "v_pk_mul_f32 v[148:149], v[132:133], v[20:21]\n"
      "v_pk_mul_f32 v[146:147], v[130:131], v[22:23]\n"
      "v_pk_mul_f32 v[150:151], v[134:135], v[22:23]\n"
      "v_pk_fma_f32 v[144:145], v[132:133], v[24:25], v[144:145]\n"
      "v_pk_fma_f32 v[148:149], v[128:129], v[24:25], v[148:149]  neg_lo:[1,0,0] neg_hi:[1,0,0]\n"
      "v_pk_fma_f32 v[146:147], v[134:135], v[26:27], v[146:147]\n"
      "v_pk_fma_f32 v[150:151], v[130:131], v[26:27], v[150:151]  neg_lo:[1,0,0] neg_hi:[1,0,0]\n"
      "v_pk_mul_f32 v[160:161], v[136:137], v[20:21]\n"
      "v_pk_mul_f32 v[164:165], v[140:141], v[20:21]\n"
      "v_pk_mul_f32 v[162:163], v[138:139], v[22:23]\n"
      "v_pk_mul_f32 v[166:167], v[142:143], v[22:23]\n"
      "v_pk_fma_f32 v[160:161], v[140:141], v[24:25], v[160:161]\n"
      "v_pk_fma_f32 v[164:165], v[136:137], v[24:25], v[164:165]  neg_lo:[1,0,0] neg_hi:[1,0,0]\n"
      "v_pk_fma_f32 v[162:163], v[142:143], v[26:27], v[162:163]\n"
      "v_pk_fma_f32 v[166:167], v[138:139], v[26:27], v[166:167]  neg_lo:[1,0,0] neg_hi:[1,0,0]\n"
      "v_cvt_pk_bf16_f32 v152, v144, v148\n"
      "v_cvt_pk_bf16_f32 v153, v145, v149\n"
      "v_cvt_pk_bf16_f32 v154, v146, v150\n"
      "v_cvt_pk_bf16_f32 v155, v147, v151\n"
      "v_cvt_pk_bf16_f32 v156, v160, v164\n"
      "v_cvt_pk_bf16_f32 v157, v161, v165\n"
      "v_mfma_f32_16x16x32_bf16 v[128:131], v[8:11], v[152:155], 0\n"
      "v_cvt_pk_bf16_f32 v158, v162, v166\n"
      "v_cvt_pk_bf16_f32 v159, v163, v167\n"
      "v_mfma_f32_16x16x32_bf16 v[132:135], v[16:19], v[152:155], 0\n"
      "s_nop 0\n"
      "v_mfma_f32_16x16x32_bf16 v[136:139], v[8:11], v[156:159], 0\n"
      "v_mfma_f32_16x16x32_bf16 v[140:143], v[16:19], v[156:159], 0\n"
      "s_add_u32 m0, s64, 2048\n"
      "v_add_u32 v124, 0xa800, v124\n"
      "global_load_lds_dwordx4 v125, s[70:71]\n"
      "s_nop 1\n"
      "v_cvt_pk_bf16_f32 v152, v128, v132\n"
      "v_cvt_pk_bf16_f32 v153, v129, v133\n"
      "v_cvt_pk_bf16_f32 v154, v130, v134\n"
      "v_cvt_pk_bf16_f32 v155, v131, v135\n"
      "ds_write_b128 v225, v[152:155] offset:0\n"
      "v_cvt_pk_bf16_f32 v156, v136, v140\n"
      "v_cvt_pk_bf16_f32 v157, v137, v141\n"
      "v_cvt_pk_bf16_f32 v158, v138, v142\n"
      "v_cvt_pk_bf16_f32 v159, v139, v143\n"
      "ds_write_b128 v225, v[156:159] offset:1280\n"
      "s_add_u32 m0, s64, 3072\n"
      "v_add_u32 v125, 0x3800, v125\n"
      "global_load_lds_dwordx4 v125, s[70:71]\n"
      "s_waitcnt lgkmcnt(0)\n"
      "s_barrier\n"
      "ds_read2st64_b32 v[160:161], v226 offset0:0 offset1:5\n"
      "ds_read2st64_b32 v[162:163], v226 offset0:10 offset1:15\n"
      "ds_read2st64_b32 v[164:165], v227 offset0:0 offset1:5\n"
      "ds_read2st64_b32 v[166:167], v227 offset0:10 offset1:15\n"
      "s_add_u32 m0, s64, 4096\n"
      "v_add_u32 v125, 0xa800, v125\n"
      "global_load_lds_dwordx4 v124, s[70:71]\n"
      "s_waitcnt lgkmcnt(2)\n"
      "v_mfma_f32_32x32x16_bf16 v[128:143], v[28:31], v[160:163], 0\n"
      "s_waitcnt lgkmcnt(0)\n"
      "v_mfma_f32_32x32x16_bf16 v[144:159], v[28:31], v[164:167], 0\n"
      "s_add_u32 m0, s64, 5120\n"
      "v_add_u32 v124, 0x3800, v124\n"
      "global_load_lds_dwordx4 v124, s[70:71]\n"
      "s_nop 7\n"
      "v_cvt_pk_bf16_f32 v160, v128, v129\n"
      "v_cvt_pk_bf16_f32 v161, v130, v131\n"
      "v_cvt_pk_bf16_f32 v162, v132, v133\n"
      "v_cvt_pk_bf16_f32 v163, v134, v135\n"
      "s_waitcnt vmcnt(10)\n"
      "global_store_dwordx4 v224, v[160:163], s[52:53] offset:0 sc1\n"
      "v_cvt_pk_bf16_f32 v164, v144, v145\n"
      "v_cvt_pk_bf16_f32 v165, v146, v147\n"
      "v_cvt_pk_bf16_f32 v166, v148, v149\n"
      "v_cvt_pk_bf16_f32 v167, v150, v151\n"
      "global_store_dwordx4 v224, v[164:167], s[52:53] offset:1024 sc1\n"
      "v_accvgpr_read_b32 v6, a236\n"
      "v_accvgpr_read_b32 v7, a237\n"
      "ds_read_b64_tr_b16 v[168:169], v114 offset:0\n"
      "ds_read_b64_tr_b16 v[170:171], v114 offset:1024\n"
      "ds_read_b64_tr_b16 v[172:173], v114 offset:128\n"
      "ds_read_b64_tr_b16 v[174:175], v114 offset:1152\n"
      "s_add_u32 m0, s64, 6144\n"
      "v_add_u32 v124, 0x26800, v124\n"
      "global_load_lds_dwordx4 v125, s[70:71]\n"
      "s_add_u32 m0, s64, 7168\n"
      "v_add_u32 v125, 0x3800, v125\n"
      "global_load_lds_dwordx4 v125, s[70:71]\n"
      "s_add_u32 m0, s64, 32768\n"
      "v_add_u32 v125, 0x26800, v125\n"
      "global_load_lds_dwordx4 v124, s[70:71]\n"
      "s_add_u32 m0, s64, 33792\n"
      "v_add_u32 v124, 0x3800, v124\n"
      "global_load_lds_dwordx4 v124, s[70:71]\n"
      "s_add_u32 m0, s64, 34816\n"
      "v_add_u32 v124, 0xa800, v124\n"
      "global_load_lds_dwordx4 v125, s[70:71]\n"
      "s_add_u32 m0, s64, 35840\n"
      "v_add_u32 v125, 0x3800, v125\n"
      "global_load_lds_dwordx4 v125, s[70:71]\n"
      "s_add_u32 m0, s64, 36864\n"
      "v_add_u32 v125, 0xa800, v125\n"
      "global_load_lds_dwordx4 v124, s[70:71]\n"
      "s_add_u32 m0, s64, 37888\n"
      "v_add_u32 v124, 0x3800, v124\n"
      "global_load_lds_dwordx4 v124, s[70:71]\n"
      "s_add_u32 m0, s64, 38912\n"
      "v_add_u32 v124, 0x26800, v124\n"
      "global_load_lds_dwordx4 v125, s[70:71]\n"
      "s_add_u32 m0, s64, 39936\n"
      "v_add_u32 v125, 0x3800, v125\n"
      "global_load_lds_dwordx4 v125, s[70:71]\n"
      "s_add_u32 m0, s64, 65536\n"
      "v_add_u32 v125, 0x26800, v125\n"
      "global_load_lds_dwordx4 v124, s[70:71]\n"
      "s_add_u32 m0, s64, 66560\n"
      "v_add_u32 v124, 0x3800, v124\n"
      "global_load_lds_dwordx4 v124, s[70:71]\n"
      "global_load_dword v200, v127, s[52:53] offset:0 sc1\n"
      "s_add_u32 m0, s64, 67584\n"
      "v_add_u32 v124, 0xa800, v124\n"
      "global_load_lds_dwordx4 v125, s[70:71]\n"
      "s_add_u32 m0, s64, 68608\n"
      "v_add_u32 v125, 0x3800, v125\n"
      "global_load_lds_dwordx4 v125, s[70:71]\n"
      "s_add_u32 m0, s64, 69632\n"
      "v_add_u32 v125, 0xa800, v125\n"
      "global_load_lds_dwordx4 v124, s[70:71]\n"
      "s_add_u32 m0, s64, 70656\n"
      "v_add_u32 v124, 0x3800, v124\n"
      "global_load_lds_dwordx4 v124, s[70:71]\n"
      "s_add_u32 m0, s64, 71680\n"
      "v_add_u32 v124, 0x26800, v124\n"
      "global_load_lds_dwordx4 v125, s[70:71]\n"
      "s_add_u32 m0, s64, 72704\n"
      "v_add_u32 v125, 0x3800, v125\n"
      "global_load_lds_dwordx4 v125, s[70:71]\n"
      "s_branch .Lq_Lsk_f\n"
      ".Lq_Rf:\n"
      "s_sub_u32 s69, s69, 1\n"
      "s_cbranch_scc1 .Lq_Tf\n"
      "s_mov_b64 exec, vcc\n"
      "global_load_dword v200, v127, s[52:53] offset:0 sc1\n"
      "s_mov_b64 exec, -1\n"
      "s_waitcnt vmcnt(0)\n"
      "v_cmp_ne_u32 vcc, s67, v200\n"
      "s_cbranch_vccnz .Lq_Rf\n"
      "s_branch .Lq_Cf\n"
      ".Lq_Tf:\n"
      "s_mov_b32 s69, 0\n"
      "s_branch .Lq_Cf\n"
      ".Lq_SP0:\n"
      "v_accvgpr_read_b32 v206, a229\n"
      "v_accvgpr_read_b32 v205, a228\n"
      "v_accvgpr_read_b32 v207, a230\n"
      "v_accvgpr_read_b32 v201, a224\n"
      "v_accvgpr_read_b32 v202, a225\n"
      "v_accvgpr_read_b32 v203, a226\n"
      "v_accvgpr_read_b32 v204, a227\n"
      "global_load_dwordx3 v[176:178], v206, s[52:53] offset:0 sc1\n"
      "global_load_dword v179, v205, s[52:53] offset:0 sc1\n"
      "global_load_dwordx3 v[180:182], v206, s[52:53] offset:1024 sc1\n"
      "global_load_dword v183, v205, s[52:53] offset:1024 sc1\n"
      "global_load_dwordx4 v[188:191], v207, s[52:53] offset:0 sc1\n"
      "s_cmp_lg_u32 s56, 0\n"
      "s_cbranch_scc1 .Lq_SP0_ret\n"
      "v_mov_b32 v0, 0x0\n"
      "global_store_dword v0, v214, s[52:53] offset:0 sc1\n"
      "s_branch .Lq_SP0_ret\n"
      ".Lq_Lsk_f:\n"
      "s_waitcnt vmcnt(6)\n"
      "v_cmp_ne_u32 vcc, s67, v200\n"
      "s_cbranch_vccnz .Lq_Rf\n"
      ".Lq_Cf:\n"
      "s_barrier\n"
      "s_cmp_eq_u32 s60, 0\n"
      "s_cbranch_scc1 .Lq_SP0\n"
      ".Lq_SP0_ret:\n"
      "global_load_dwordx4 v[116:119], v234, s[52:53] offset:0 sc1\n"
      "global_load_dwordx4 v[120:123], v234, s[52:53] offset:1024 sc1\n"
      "s_branch .Lq_Lsk_s\n"
      ".Lq_SPa:\n"
      "s_mov_b32 s72, 0x35000000\n"
      "s_mov_b32 s73, 0x35000000\n"
      "s_mov_b32 s74, 0x35000000\n"
      "v_lshlrev_b32 v144, 16, v179\n"
      "v_and_b32 v148, 0xffff0000, v179\n"
      "v_lshlrev_b32 v145, 16, v178\n"
      "v_and_b32 v149, 0xffff0000, v178\n"
      "v_lshlrev_b32 v146, 16, v177\n"
      "v_and_b32 v150, 0xffff0000, v177\n"
      "v_lshlrev_b32 v147, 16, v176\n"
      "v_and_b32 v151, 0xffff0000, v176\n"
      "v_pk_add_f32 v[128:129], v[128:129], v[144:145]\n"
      "v_pk_add_f32 v[132:133], v[132:133], v[148:149] neg_lo:[0,1] neg_hi:[0,1]\n"
      "v_pk_add_f32 v[130:131], v[130:131], v[146:147]\n"
      "v_pk_add_f32 v[134:135], v[134:135], v[150:151] neg_lo:[0,1] neg_hi:[0,1]\n"
      "v_lshlrev_b32 v144, 16, v183\n"
      "v_and_b32 v148, 0xffff0000, v183\n"
      "v_lshlrev_b32 v145, 16, v182\n"
      "v_and_b32 v149, 0xffff0000, v182\n"
      "v_lshlrev_b32 v146, 16, v181\n"
      "v_and_b32 v150, 0xffff0000, v181\n"
      "v_lshlrev_b32 v147, 16, v180\n"
      "v_and_b32 v151, 0xffff0000, v180\n"
      "v_pk_add_f32 v[136:137], v[136:137], v[144:145]\n"
      "v_pk_add_f32 v[140:141], v[140:141], v[148:149] neg_lo:[0,1] neg_hi:[0,1]\n"
      "v_pk_add_f32 v[138:139], v[138:139], v[146:147]\n"
      "v_pk_add_f32 v[142:143], v[142:143], v[150:151] neg_lo:[0,1] neg_hi:[0,1]\n"
      "s_branch .Lq_SPa_ret\n"
      ".Lq_SPb:\n"
      "v_lshlrev_b32 v108, 16, v188\n"
      "v_and_b32 v112, 0xffff0000, v188\n"
      "v_lshlrev_b32 v109, 16, v189\n"
      "v_and_b32 v113, 0xffff0000, v189\n"
      "v_lshlrev_b32 v110, 16, v190\n"
      "v_and_b32 v114, 0xffff0000, v190\n"
      "v_lshlrev_b32 v111, 16, v191\n"
      "v_and_b32 v115, 0xffff0000, v191\n"
      "v_pk_mul_f32 v[108:109], v[108:109], s[74:75] op_sel:[0,1] op_sel_hi:[1,1]\n"
      "v_pk_mul_f32 v[110:111], v[110:111], s[74:75] op_sel:[0,1] op_sel_hi:[1,1]\n"
      "v_pk_mul_f32 v[112:113], v[112:113], s[74:75] op_sel:[0,1] op_sel_hi:[1,1]\n"
      "v_pk_mul_f32 v[114:115], v[114:115], s[74:75] op_sel:[0,1] op_sel_hi:[1,1]\n"
      "s_branch .Lq_SPb_ret\n"
      ".Lq_SPc:\n"
      "global_load_dwordx4 v[192:195], v202, s[52:53] offset:0 sc1\n"
      "global_load_dwordx4 v[196:199], v202, s[86:87] offset:0 sc1\n"
      "v_mov_b32 v228, v201\n"
      "v_mov_b32 v205, v229\n"
      "v_mov_b32 v229, v203\n"
      "s_branch .Lq_SPc_ret\n"
      ".Lq_Lsk_s:\n"
      "s_cmp_eq_u32 s60, 0\n"
      "s_cbranch_scc1 .Lq_SPc\n"
      ".Lq_SPc_ret:\n"
      "global_load_dwordx4 v[160:163], v228, s[52:53] offset:0 sc1\n"
      "global_load_dwordx4 v[164:167], v228, s[86:87] offset:0 sc1\n"
      "ds_read_b64_tr_b16 a[0:1], v236 offset:0\n"
      "ds_read_b64_tr_b16 a[2:3], v236 offset:1024\n"
      "ds_read_b64_tr_b16 a[4:5], v237 offset:0\n"
      "ds_read_b64_tr_b16 a[6:7], v237 offset:1024\n"
      "ds_read_b64_tr_b16 a[8:9], v238 offset:0\n"
      "ds_read_b64_tr_b16 a[10:11], v238 offset:1024\n"
      "ds_read_b64_tr_b16 a[12:13], v239 offset:0\n"
      "ds_read_b64_tr_b16 a[14:15], v239 offset:1024\n"
      "ds_read_b64_tr_b16 a[16:17], v236 offset:16384\n"
      "ds_read_b64_tr_b16 a[18:19], v236 offset:17408\n"
      "ds_read_b64_tr_b16 a[20:21], v237 offset:16384\n"
      "ds_read_b64_tr_b16 a[22:23], v237 offset:17408\n"
      "ds_read_b64_tr_b16 a[24:25], v238 offset:16384\n"
      "ds_read_b64_tr_b16 a[26:27], v238 offset:17408\n"
      "ds_read_b64_tr_b16 a[28:29], v239 offset:16384\n"
      "ds_read_b64_tr_b16 a[30:31], v239 offset:17408\n"
      "ds_read_b64_tr_b16 a[32:33], v236 offset:32768\n"
      "ds_read_b64_tr_b16 a[34:35], v236 offset:33792\n"
      "ds_read_b64_tr_b16 a[36:37], v237 offset:32768\n"
      "ds_read_b64_tr_b16 a[38:39], v237 offset:33792\n"
      "ds_read_b64_tr_b16 a[40:41], v238 offset:32768\n"
      "ds_read_b64_tr_b16 a[42:43], v238 offset:33792\n"
      "ds_read_b64_tr_b16 a[44:45], v239 offset:32768\n"
      "ds_read_b64_tr_b16 a[46:47], v239 offset:33792\n"
      "ds_read_b64_tr_b16 a[48:49], v236 offset:49152\n"
      "ds_read_b64_tr_b16 a[50:51], v236 offset:50176\n"
      "ds_read_b64_tr_b16 a[52:53], v237 offset:49152\n"
      "ds_read_b64_tr_b16 a[54:55], v237 offset:50176\n"
      "ds_read_b64_tr_b16 a[56:57], v238 offset:49152\n"
      "ds_read_b64_tr_b16 a[58:59], v238 offset:50176\n"
      "ds_read_b64_tr_b16 a[60:61], v239 offset:49152\n"
      "ds_read_b64_tr_b16 a[62:63], v239 offset:50176\n"
      "v_xor_b32 v236, 0x10000, v236\n"
      "v_xor_b32 v237, 0x10000, v237\n"
      "v_xor_b32 v238, 0x10000, v238\n"
      "v_xor_b32 v239, 0x10000, v239\n"
      "s_waitcnt vmcnt(63) expcnt(7) lgkmcnt(15)\n"
      "s_branch .Lq_Lsk_u2\n"
      ".Lq_SPf:\n"
      "v_pk_mul_f32 v[184:185], v[176:177], v[40:41]\n"
      "v_pk_mul_f32 v[188:189], v[180:181], v[40:41]\n"
      "v_pk_mul_f32 v[186:187], v[178:179], v[42:43]\n"
      "v_pk_mul_f32 v[190:191], v[182:183], v[42:43]\n"
      "v_pk_fma_f32 v[184:185], v[180:181], v[44:45], v[184:185]\n"
      "v_pk_fma_f32 v[188:189], v[176:177], v[44:45], v[188:189]  neg_lo:[1,0,0] neg_hi:[1,0,0]\n"
      "v_pk_fma_f32 v[186:187], v[182:183], v[46:47], v[186:187]\n"
      "v_pk_fma_f32 v[190:191], v[178:179], v[46:47], v[190:191]  neg_lo:[1,0,0] neg_hi:[1,0,0]\n"
      "v_cvt_pk_bf16_f32 v192, v184, v188\n"
      "v_cvt_pk_bf16_f32 v193, v185, v189\n"
      "v_cvt_pk_bf16_f32 v194, v186, v190\n"
      "v_cvt_pk_bf16_f32 v195, v187, v191\n"
      "s_nop 1\n"
      "v_mfma_f32_16x16x32_bf16 v[176:179], v[8:11], v[192:195], 0\n"
      "v_mfma_f32_16x16x32_bf16 v[180:183], v[12:15], v[192:195], 0\n"
      "s_branch .Lq_SPf_ret\n"
      ".Lq_SPg:\n"
      "s_nop 0\n"
      "s_nop 4\n"
      "v_pk_fma_f32 v[144:145], v[100:101], v[176:177], v[144:145]\n"
      "v_pk_fma_f32 v[148:149], v[100:101], v[180:181], v[148:149]\n"
      "v_pk_fma_f32 v[146:147], v[102:103], v[178:179], v[146:147]\n"
      "v_pk_fma_f32 v[150:151], v[102:103], v[182:183], v[150:151]\n"
      "v_pk_fma_f32 v[144:145], v[104:105], v[180:181], v[144:145]  neg_lo:[1,0,0] neg_hi:[1,0,0]\n"
      "v_pk_fma_f32 v[148:149], v[104:105], v[176:177], v[148:149]\n"
      "v_pk_fma_f32 v[146:147], v[106:107], v[182:183], v[146:147]  neg_lo:[1,0,0] neg_hi:[1,0,0]\n"
      "v_pk_fma_f32 v[150:151], v[106:107], v[178:179], v[150:151]\n"
      "s_branch .Lq_SPg_ret\n"
      ".Lq_Lsk_u2:\n"
      "s_waitcnt vmcnt(3)\n"
      "v_lshlrev_b32 v128, 16, v116\n"
      "v_and_b32 v132, 0xffff0000, v116\n"
      "v_lshlrev_b32 v129, 16, v117\n"
      "v_and_b32 v133, 0xffff0000, v117\n"
      "v_lshlrev_b32 v130, 16, v118\n"
      "v_and_b32 v134, 0xffff0000, v118\n"
      "v_lshlrev_b32 v131, 16, v119\n"
      "v_and_b32 v135, 0xffff0000, v119\n"
      "s_waitcnt vmcnt(2)\n"
      "v_lshlrev_b32 v136, 16, v120\n"
      "v_and_b32 v140, 0xffff0000, v120\n"
      "v_lshlrev_b32 v137, 16, v121\n"
      "v_and_b32 v141, 0xffff0000, v121\n"
      "v_lshlrev_b32 v138, 16, v122\n"
      "v_and_b32 v142, 0xffff0000, v122\n"
      "v_lshlrev_b32 v139, 16, v123\n"
      "v_and_b32 v143, 0xffff0000, v123\n"
      "s_cmp_eq_u32 s60, 0\n"
      "s_waitcnt vmcnt(63) expcnt(7) lgkmcnt(15)\n"
      "s_cbranch_scc1 .Lq_SPa\n"
      ".Lq_SPa_ret:\n"
      "v_pk_mul_f32 v[92:93], v[128:129], s[72:73] op_sel:[0,0] op_sel_hi:[1,0]\n"
      "v_pk_mul_f32 v[96:97], v[132:133], s[72:73] op_sel:[0,0] op_sel_hi:[1,0]\n"
      "v_pk_mul_f32 v[108:109], v[128:129], s[72:73] op_sel:[0,1] op_sel_hi:[1,1]\n"
      "v_pk_mul_f32 v[112:113], v[132:133], s[72:73] op_sel:[0,1] op_sel_hi:[1,1]\n"
      "v_pk_add_f32 v[100:101], v[132:133], v[140:141]\n"
      "v_pk_add_f32 v[104:105], v[128:129], v[136:137] neg_lo:[0,1] neg_hi:[0,1]\n"
      "v_pk_fma_f32 v[92:93], v[136:137], s[72:73], v[92:93] op_sel:[0,1,0] op_sel_hi:[1,1,1]\n"
      "v_pk_fma_f32 v[96:97], v[140:141], s[72:73], v[96:97] op_sel:[0,1,0] op_sel_hi:[1,1,1] neg_lo:[1,0,0] neg_hi:[1,0,0]\n"
      "v_pk_fma_f32 v[108:109], v[136:137], s[72:73], v[108:109] op_sel:[0,0,0] op_sel_hi:[1,0,1]\n"
      "v_pk_fma_f32 v[112:113], v[140:141], s[72:73], v[112:113] op_sel:[0,0,0] op_sel_hi:[1,0,1] neg_lo:[1,0,0] neg_hi:[1,0,0]\n"
      "v_pk_mul_f32 v[100:101], v[100:101], s[74:75] op_sel:[0,0] op_sel_hi:[1,0] neg_lo:[1,0] neg_hi:[1,0]\n"
      "v_pk_mul_f32 v[104:105], v[104:105], s[74:75] op_sel:[0,0] op_sel_hi:[1,0]\n"
      "v_pk_mul_f32 v[94:95], v[130:131], s[72:73] op_sel:[0,0] op_sel_hi:[1,0]\n"
      "v_pk_mul_f32 v[98:99], v[134:135], s[72:73] op_sel:[0,0] op_sel_hi:[1,0]\n"
      "v_pk_mul_f32 v[110:111], v[130:131], s[72:73] op_sel:[0,1] op_sel_hi:[1,1]\n"
      "v_pk_mul_f32 v[114:115], v[134:135], s[72:73] op_sel:[0,1] op_sel_hi:[1,1]\n"
      "v_pk_add_f32 v[102:103], v[134:135], v[142:143]\n"
      "v_pk_add_f32 v[106:107], v[130:131], v[138:139] neg_lo:[0,1] neg_hi:[0,1]\n"
      "v_pk_fma_f32 v[94:95], v[138:139], s[72:73], v[94:95] op_sel:[0,1,0] op_sel_hi:[1,1,1]\n"
      "v_pk_fma_f32 v[98:99], v[142:143], s[72:73], v[98:99] op_sel:[0,1,0] op_sel_hi:[1,1,1] neg_lo:[1,0,0] neg_hi:[1,0,0]\n"
      "v_pk_fma_f32 v[110:111], v[138:139], s[72:73], v[110:111] op_sel:[0,0,0] op_sel_hi:[1,0,1]\n"
      "v_pk_fma_f32 v[114:115], v[142:143], s[72:73], v[114:115] op_sel:[0,0,0] op_sel_hi:[1,0,1] neg_lo:[1,0,0] neg_hi:[1,0,0]\n"
      "v_pk_mul_f32 v[102:103], v[102:103], s[74:75] op_sel:[0,0] op_sel_hi:[1,0] neg_lo:[1,0] neg_hi:[1,0]\n"
      "v_pk_mul_f32 v[106:107], v[106:107], s[74:75] op_sel:[0,0] op_sel_hi:[1,0]\n"
      "s_cmp_eq_u32 s60, 0\n"
      "s_cbranch_scc1 .Lq_SPb\n"
      ".Lq_SPb_ret:\n"
      "global_store_dwordx4 v231, a[252:255], s[92:93] offset:-4096 sc1\n"
      "global_store_dwordx4 v231, a[252:255], s[92:93] offset:0 sc1\n"
      "s_branch .Lq_Lsk_u\n"
      ".Lq_Ru:\n"
      "s_sub_u32 s69, s69, 1\n"
      "s_cbranch_scc1 .Lq_Tu\n"
      "s_mov_b64 exec, vcc\n"
      "global_load_dwordx4 v[160:163], v228, s[52:53] offset:0 sc1\n"
      "global_load_dwordx4 v[164:167], v228, s[86:87] offset:0 sc1\n"
      "s_mov_b64 exec, -1\n"
      "s_waitcnt vmcnt(0)\n"
      "v_max3_u32 v144, v160, v161, v162\n"
      "v_max3_u32 v145, v163, v164, v165\n"
      "v_max3_u32 v144, v144, v166, v167\n"
      "v_max_u32 v144, v144, v145\n"
      "v_cmp_eq_u32 vcc, -1, v144\n"
      "s_cbranch_vccnz .Lq_Ru\n"
      "s_branch .Lq_Cu\n"
      ".Lq_Tu:\n"
      "s_mov_b32 s69, 0\n"
      "s_branch .Lq_Cu\n"
      ".Lq_SPd:\n"
      "v_max3_u32 v144, v192, v193, v194\n"
      "v_max3_u32 v145, v195, v196, v197\n"
      "v_max3_u32 v144, v144, v198, v199\n"
      "v_max_u32 v144, v144, v145\n"
      "v_cmp_eq_u32 vcc, -1, v144\n"
      "s_cbranch_vccz .Lq_SPd_ok\n"
      "s_sub_u32 s69, s69, 1\n"
      "s_cbranch_scc1 .Lq_SPd_to\n"
      "s_mov_b64 exec, vcc\n"
      "global_load_dwordx4 v[192:195], v202, s[52:53] offset:0 sc1\n"
      "global_load_dwordx4 v[196:199], v202, s[86:87] offset:0 sc1\n"
      "s_mov_b64 exec, -1\n"
      "s_waitcnt vmcnt(0)\n"
      "s_branch .Lq_SPd\n"
      ".Lq_SPd_to:\n"
      "s_mov_b32 s69, 0\n"
      ".Lq_SPd_ok:\n"
      "ds_write2st64_b32 v229, v192, v193 offset0:1 offset1:5\n"
      "ds_write2st64_b32 v229, v194, v195 offset0:9 offset1:13\n"
      "ds_write2st64_b32 v229, v196, v197 offset0:3 offset1:7\n"
      "ds_write2st64_b32 v229, v198, v199 offset0:11 offset1:15\n"
      "v_mov_b32 v229, v205\n"
      "s_branch .Lq_SPd_ret\n"
      ".Lq_SPe:\n"
      "ds_read_b128 v[192:195], v204\n"
      "s_waitcnt lgkmcnt(0)\n"
      "v_xor_b32 v192, 0x80000000, v192\n"
      "v_xor_b32 v193, 0x80000000, v193\n"
      "v_xor_b32 v194, 0x80000000, v194\n"
      "v_xor_b32 v195, 0x80000000, v195\n"
      "s_nop 1\n"
      "v_mfma_f32_16x16x32_bf16 v[176:179], v[192:195], v[32:35], 0\n"
      "v_mfma_f32_16x16x32_bf16 v[180:183], v[192:195], v[36:39], 0\n"
      "s_branch .Lq_SPe_ret\n"
      ".Lq_Lsk_u:\n"
      "s_waitcnt vmcnt(3)\n"
      "v_max3_u32 v144, v160, v161, v162\n"
      "s_waitcnt vmcnt(2)\n"
      "v_max3_u32 v145, v163, v164, v165\n"
      "v_max3_u32 v144, v144, v166, v167\n"
      "v_max_u32 v144, v144, v145\n"
      "v_cmp_eq_u32 vcc, -1, v144\n"
      "s_cbranch_vccnz .Lq_Ru\n"
      ".Lq_Cu:\n"
      "ds_write2st64_b32 v229, v160, v161 offset0:0 offset1:4\n"
      "ds_write2st64_b32 v229, v162, v163 offset0:8 offset1:12\n"
      "ds_write2st64_b32 v229, v164, v165 offset0:2 offset1:6\n"
      "ds_write2st64_b32 v229, v166, v167 offset0:10 offset1:14\n"
      "s_cmp_eq_u32 s60, 0\n"
      "s_cbranch_scc1 .Lq_SPd\n"
      ".Lq_SPd_ret:\n"
      "s_waitcnt lgkmcnt(15)\n"
      "global_store_dwordx4 v6, v[168:171], s[52:53] offset:0 sc1\n"
      "global_store_dwordx4 v7, v[172:175], s[52:53] offset:0 sc1\n"
      "s_waitcnt lgkmcnt(0)\n"
      "s_barrier\n"
      "ds_read_b128 v[152:155], v230\n"
      "ds_read_b128 v[156:159], v230 offset:4160\n"
      "s_cmp_eq_u32 s60, 0\n"
      "s_cbranch_scc1 .Lq_SPe\n"
      ".Lq_SPe_ret:\n"
      "s_waitcnt lgkmcnt(1)\n"
      "v_mfma_f32_16x16x32_bf16 v[128:131], v[152:155], v[32:35], 0\n"
      "v_mfma_f32_16x16x32_bf16 v[132:135], v[152:155], v[36:39], 0\n"
      "s_waitcnt lgkmcnt(0)\n"
      "v_mfma_f32_16x16x32_bf16 v[136:139], v[156:159], v[32:35], 0\n"
      "v_mfma_f32_16x16x32_bf16 v[140:143], v[156:159], v[36:39], 0\n"
      "s_add_u32 m0, s64, 98304\n"
      "v_add_u32 v125, 0x26800, v125\n"
      "global_load_lds_dwordx4 v124, s[70:71]\n"
      "s_nop 1\n"
      "v_pk_mul_f32 v[144:145], v[128:129], v[40:41]\n"
      "v_pk_mul_f32 v[148:149], v[132:133], v[40:41]\n"
      "v_pk_mul_f32 v[146:147], v[130:131], v[42:43]\n"
      "v_pk_mul_f32 v[150:151], v[134:135], v[42:43]\n"
      "v_pk_fma_f32 v[144:145], v[132:133], v[44:45], v[144:145]\n"
      "v_pk_fma_f32 v[148:149], v[128:129], v[44:45], v[148:149]  neg_lo:[1,0,0] neg_hi:[1,0,0]\n"
      "v_pk_fma_f32 v[146:147], v[134:135], v[46:47], v[146:147]\n"
      "v_pk_fma_f32 v[150:151], v[130:131], v[46:47], v[150:151]  neg_lo:[1,0,0] neg_hi:[1,0,0]\n"
      "v_pk_mul_f32 v[160:161], v[136:137], v[40:41]\n"
      "v_pk_mul_f32 v[164:165], v[140:141], v[40:41]\n"
      "v_pk_mul_f32 v[162:163], v[138:139], v[42:43]\n"
      "v_pk_mul_f32 v[166:167], v[142:143], v[42:43]\n"
      "v_pk_fma_f32 v[160:161], v[140:141], v[44:45], v[160:161]\n"
      "v_pk_fma_f32 v[164:165], v[136:137], v[44:45], v[164:165]  neg_lo:[1,0,0] neg_hi:[1,0,0]\n"
      "v_pk_fma_f32 v[162:163], v[142:143], v[46:47], v[162:163]\n"
      "v_pk_fma_f32 v[166:167], v[138:139], v[46:47], v[166:167]  neg_lo:[1,0,0] neg_hi:[1,0,0]\n"
      "s_cmp_eq_u32 s60, 0\n"
      "s_cbranch_scc1 .Lq_SPf\n"
      ".Lq_SPf_ret:\n"
      "v_cvt_pk_bf16_f32 v152, v144, v148\n"
      "v_cvt_pk_bf16_f32 v153, v145, v149\n"
      "v_cvt_pk_bf16_f32 v154, v146, v150\n"
      "v_cvt_pk_bf16_f32 v155, v147, v151\n"
      "v_cvt_pk_bf16_f32 v156, v160, v164\n"
      "v_cvt_pk_bf16_f32 v157, v161, v165\n"
      "v_mfma_f32_16x16x32_bf16 v[128:131], v[8:11], v[152:155], 0\n"
      "v_cvt_pk_bf16_f32 v158, v162, v166\n"
      "v_cvt_pk_bf16_f32 v159, v163, v167\n"
      "v_mfma_f32_16x16x32_bf16 v[132:135], v[12:15], v[152:155], 0\n"
      "s_nop 0\n"
      "v_mfma_f32_16x16x32_bf16 v[136:139], v[8:11], v[156:159], 0\n"
      "v_mfma_f32_16x16x32_bf16 v[140:143], v[12:15], v[156:159], 0\n"
      "s_add_u32 m0, s64, 99328\n"
      "v_add_u32 v124, 0x3800, v124\n"
      "global_load_lds_dwordx4 v124, s[70:71]\n"
      "v_pk_mul_f32 v[144:145], v[92:93], v[128:129]\n"
      "s_nop 0\n"
      "v_pk_mul_f32 v[148:149], v[92:93], v[132:133]\n"
      "v_pk_mul_f32 v[146:147], v[94:95], v[130:131]\n"
      "v_pk_mul_f32 v[150:151], v[94:95], v[134:135]\n"
      "v_pk_fma_f32 v[144:145], v[96:97], v[132:133], v[144:145]  neg_lo:[1,0,0] neg_hi:[1,0,0]\n"
      "v_pk_fma_f32 v[148:149], v[96:97], v[128:129], v[148:149]\n"
      "v_pk_fma_f32 v[146:147], v[98:99], v[134:135], v[146:147]  neg_lo:[1,0,0] neg_hi:[1,0,0]\n"
      "v_pk_fma_f32 v[150:151], v[98:99], v[130:131], v[150:151]\n"
      "v_pk_mul_f32 v[160:161], v[108:109], v[136:137]\n"
      "v_pk_mul_f32 v[164:165], v[108:109], v[140:141]\n"
      "v_pk_mul_f32 v[162:163], v[110:111], v[138:139]\n"
      "v_pk_mul_f32 v[166:167], v[110:111], v[142:143]\n"
      "v_pk_fma_f32 v[160:161], v[112:113], v[140:141], v[160:161]  neg_lo:[1,0,0] neg_hi:[1,0,0]\n"
      "v_pk_fma_f32 v[164:165], v[112:113], v[136:137], v[164:165]\n"
      "v_pk_fma_f32 v[162:163], v[114:115], v[142:143], v[162:163]  neg_lo:[1,0,0] neg_hi:[1,0,0]\n"
      "v_pk_fma_f32 v[166:167], v[114:115], v[138:139], v[166:167]\n"
      "s_add_u32 m0, s64, 100352\n"
      "v_add_u32 v124, 0xa800, v124\n"
      "global_load_lds_dwordx4 v125, s[70:71]\n"
      "s_cmp_eq_u32 s60, 0\n"
      "s_cbranch_scc1 .Lq_SPg\n"
      "v_pk_fma_f32 v[144:145], v[100:101], v[136:137], v[144:145]\n"
      "v_pk_fma_f32 v[148:149], v[100:101], v[140:141], v[148:149]\n"
      "v_pk_fma_f32 v[146:147], v[102:103], v[138:139], v[146:147]\n"
      "v_pk_fma_f32 v[150:151], v[102:103], v[142:143], v[150:151]\n"
      "v_pk_fma_f32 v[144:145], v[104:105], v[140:141], v[144:145]  neg_lo:[1,0,0] neg_hi:[1,0,0]\n"
      "v_pk_fma_f32 v[148:149], v[104:105], v[136:137], v[148:149]\n"
      "v_pk_fma_f32 v[146:147], v[106:107], v[142:143], v[146:147]  neg_lo:[1,0,0] neg_hi:[1,0,0]\n"
      "v_pk_fma_f32 v[150:151], v[106:107], v[138:139], v[150:151]\n"
      "v_pk_fma_f32 v[160:161], v[100:101], v[128:129], v[160:161]  neg_lo:[1,0,0] neg_hi:[1,0,0]\n"
      "v_pk_fma_f32 v[164:165], v[100:101], v[132:133], v[164:165]  neg_lo:[1,0,0] neg_hi:[1,0,0]\n"
      "v_pk_fma_f32 v[162:163], v[102:103], v[130:131], v[162:163]  neg_lo:[1,0,0] neg_hi:[1,0,0]\n"
      "v_pk_fma_f32 v[166:167], v[102:103], v[134:135], v[166:167]  neg_lo:[1,0,0] neg_hi:[1,0,0]\n"
      "v_pk_fma_f32 v[160:161], v[104:105], v[132:133], v[160:161]\n"
      "v_pk_fma_f32 v[164:165], v[104:105], v[128:129], v[164:165]  neg_lo:[1,0,0] neg_hi:[1,0,0]\n"
      "v_pk_fma_f32 v[162:163], v[106:107], v[134:135], v[162:163]\n"
      "v_pk_fma_f32 v[166:167], v[106:107], v[130:131], v[166:167]  neg_lo:[1,0,0] neg_hi:[1,0,0]\n"
      ".Lq_SPg_ret:\n"
      "v_cvt_pk_bf16_f32 v152, v144, v148\n"
      "v_cvt_pk_bf16_f32 v153, v145, v149\n"
      "v_cvt_pk_bf16_f32 v154, v146, v150\n"
      "v_cvt_pk_bf16_f32 v155, v147, v151\n"
      "v_cvt_pk_bf16_f32 v156, v160, v164\n"
      "v_cvt_pk_bf16_f32 v157, v161, v165\n"
      "v_mfma_f32_16x16x32_bf16 v[128:131], v[152:155], v[48:51], 0\n"
      "v_cvt_pk_bf16_f32 v158, v162, v166\n"
      "v_cvt_pk_bf16_f32 v159, v163, v167\n"
      "v_mfma_f32_16x16x32_bf16 v[132:135], v[152:155], v[52:55], 0\n"
      "s_nop 0\n"
      "v_mfma_f32_16x16x32_bf16 v[136:139], v[156:159], v[48:51], 0\n"
      "v_mfma_f32_16x16x32_bf16 v[140:143], v[156:159], v[52:55], 0\n"
      "s_add_u32 m0, s64, 101376\n"
      "v_add_u32 v125, 0x3800, v125\n"
      "global_load_lds_dwordx4 v125, s[70:71]\n"
      "v_pk_mul_f32 v[144:145], v[128:129], v[56:57]\n"
      "s_nop 0\n"
      "v_pk_mul_f32 v[148:149], v[132:133], v[56:57]\n"
      "v_pk_mul_f32 v[146:147], v[130:131], v[58:59]\n"
      "v_pk_mul_f32 v[150:151], v[134:135], v[58:59]\n"
      "v_pk_fma_f32 v[144:145], v[132:133], v[60:61], v[144:145]  neg_lo:[1,0,0] neg_hi:[1,0,0]\n"
      "v_pk_fma_f32 v[148:149], v[128:129], v[60:61], v[148:149]\n"
      "v_pk_fma_f32 v[146:147], v[134:135], v[62:63], v[146:147]  neg_lo:[1,0,0] neg_hi:[1,0,0]\n"
      "v_pk_fma_f32 v[150:151], v[130:131], v[62:63], v[150:151]\n"
      "v_pk_mul_f32 v[160:161], v[136:137], v[56:57]\n"
      "v_pk_mul_f32 v[164:165], v[140:141], v[56:57]\n"
      "v_pk_mul_f32 v[162:163], v[138:139], v[58:59]\n"
      "v_pk_mul_f32 v[166:167], v[142:143], v[58:59]\n"
      "v_pk_fma_f32 v[160:161], v[140:141], v[60:61], v[160:161]  neg_lo:[1,0,0] neg_hi:[1,0,0]\n"
      "v_pk_fma_f32 v[164:165], v[136:137], v[60:61], v[164:165]\n"
      "v_pk_fma_f32 v[162:163], v[142:143], v[62:63], v[162:163]  neg_lo:[1,0,0] neg_hi:[1,0,0]\n"
      "v_pk_fma_f32 v[166:167], v[138:139], v[62:63], v[166:167]\n"
      "v_cvt_pk_bf16_f32 v152, v144, v148\n"
      "v_cvt_pk_bf16_f32 v153, v145, v149\n"
      "v_cvt_pk_bf16_f32 v154, v146, v150\n"
      "v_cvt_pk_bf16_f32 v155, v147, v151\n"
      "v_cvt_pk_bf16_f32 v156, v160, v164\n"
      "v_cvt_pk_bf16_f32 v157, v161, v165\n"
      "v_mfma_f32_16x16x32_bf16 v[128:131], v[64:67], v[152:155], 0\n"
      "v_cvt_pk_bf16_f32 v158, v162, v166\n"
      "v_cvt_pk_bf16_f32 v159, v163, v167\n"
      "v_mfma_f32_16x16x32_bf16 v[132:135], v[68:71], v[152:155], 0\n"
      "s_nop 0\n"
      "v_mfma_f32_16x16x32_bf16 v[136:139], v[64:67], v[156:159], 0\n"
      "v_mfma_f32_16x16x32_bf16 v[140:143], v[68:71], v[156:159], 0\n"
      "s_add_u32 m0, s64, 102400\n"
      "v_add_u32 v125, 0xa800, v125\n"
      "global_load_lds_dwordx4 v124, s[70:71]\n"
      "s_nop 1\n"
      "v_cvt_pk_bf16_f32 v152, v128, v132\n"
      "v_cvt_pk_bf16_f32 v153, v129, v133\n"
      "v_cvt_pk_bf16_f32 v154, v130, v134\n"
      "v_cvt_pk_bf16_f32 v155, v131, v135\n"
      "ds_write_b128 v230, v[152:155] offset:0\n"
      "v_cvt_pk_bf16_f32 v156, v136, v140\n"
      "v_cvt_pk_bf16_f32 v157, v137, v141\n"
      "v_cvt_pk_bf16_f32 v158, v138, v142\n"
      "v_cvt_pk_bf16_f32 v159, v139, v143\n"
      "ds_write_b128 v230, v[156:159] offset:4160\n"
      "s_add_u32 m0, s64, 103424\n"
      "v_add_u32 v124, 0x3800, v124\n"
      "global_load_lds_dwordx4 v124, s[70:71]\n"
      "s_waitcnt vmcnt(6)\n"
      "s_waitcnt lgkmcnt(0)\n"
      "s_barrier\n"
      "ds_read2st64_b32 v[160:161], v229 offset0:0 offset1:4\n"
      "ds_read2st64_b32 v[162:163], v229 offset0:8 offset1:12\n"
      "ds_read2st64_b32 v[164:165], v229 offset0:2 offset1:6\n"
      "ds_read2st64_b32 v[166:167], v229 offset0:10 offset1:14\n"
      "s_waitcnt lgkmcnt(2)\n"
      "global_store_dwordx4 v231, v[160:163], s[90:91] offset:-4096 sc1\n"
      "s_waitcnt lgkmcnt(0)\n"
      "global_store_dwordx4 v231, v[164:167], s[90:91] offset:0 sc1\n"
      "global_load_dwordx4 v[128:131], v208, s[52:53] offset:0 sc1\n"
      "global_load_dwordx4 v[132:135], v208, s[80:81] offset:0 sc1\n"
      "global_load_dwordx4 v[136:139], v208, s[82:83] offset:0 sc1\n"
      "global_load_dwordx4 v[140:143], v208, s[84:85] offset:0 sc1\n"
      "v_lshlrev_b32 v168, 16, v84\n"
      "v_and_b32 v172, 0xffff0000, v84\n"
      "v_lshlrev_b32 v169, 16, v85\n"
      "v_and_b32 v173, 0xffff0000, v85\n"
      "v_lshlrev_b32 v170, 16, v86\n"
      "v_and_b32 v174, 0xffff0000, v86\n"
      "v_lshlrev_b32 v171, 16, v87\n"
      "v_and_b32 v175, 0xffff0000, v87\n"
      "v_lshlrev_b32 v176, 16, v88\n"
      "v_and_b32 v180, 0xffff0000, v88\n"
      "v_lshlrev_b32 v177, 16, v89\n"
      "v_and_b32 v181, 0xffff0000, v89\n"
      "s_add_u32 m0, s64, 104448\n"
      "v_add_u32 v124, 0x26800, v124\n"
      "global_load_lds_dwordx4 v125, s[70:71]\n"
      "v_lshlrev_b32 v178, 16, v90\n"
      "v_and_b32 v182, 0xffff0000, v90\n"
      "v_lshlrev_b32 v179, 16, v91\n"
      "v_and_b32 v183, 0xffff0000, v91\n"
      "s_mul_i32 s4, s57, 0x4600\n"
      "v_lshrrev_b32_e32 v149, 4, v223\n"
      "v_lshl_or_b32 v149, s57, 2, v149\n"
      "v_bfe_u32 v150, v223, 3, 1\n"
      "v_lshl_or_b32 v151, v150, 4, v149\n"
      "v_bfe_u32 v153, v223, 2, 1\n"
      "v_and_b32_e32 v155, 3, v223\n"
      "v_and_b32_e32 v145, 31, v223\n"
      "s_add_u32 m0, s64, 105472\n"
      "v_add_u32 v125, 0x3800, v125\n"
      "global_load_lds_dwordx4 v125, s[70:71]\n"
      "v_lshrrev_b32_e32 v146, 5, v223\n"
      "v_mul_u32_u24_e32 v153, 0x1180, v153\n"
      "v_lshlrev_b32_e32 v154, 4, v151\n"
      "v_mul_u32_u24_e32 v155, 0x220, v155\n"
      "v_add3_u32 v216, v155, v153, v154\n"
      "v_mul_u32_u24_e32 v146, 0x220, v146\n"
      "v_lshlrev_b32_e32 v153, 4, v145\n"
      "s_lshl_b32 s0, s96, 1\n"
      "s_lshr_b32 s1, s97, 3\n"
      "v_add3_u32 v217, v146, s4, v153\n"
      "s_lshl_b32 s4, s97, 7\n"
      "s_or_b32 s2, s0, s1\n"
      "s_add_u32 m0, s64, 0\n"
      "v_add_u32 v125, 0x26800, v125\n"
      "global_load_lds_dwordx4 v124, s[70:71]\n"
      "v_and_b32_e32 v148, 7, v223\n"
      "s_and_b32 s4, s4, 0x380\n"
      "v_lshlrev_b32_e32 v149, 8, v149\n"
      "v_lshl_or_b32 v146, v148, 4, s4\n"
      "s_lshl_b32 s4, s2, 1\n"
      "v_and_b32_e32 v149, 0x700, v149\n"
      "v_lshl_or_b32 v152, s2, 6, v151\n"
      "v_and_or_b32 v149, s4, 14, v149\n"
      "s_lshl_b32 s4, s98, 11\n"
      "s_lshl_b32 s5, s57, 9\n"
      "v_lshl_or_b32 v153, v152, 10, v146\n"
      "v_lshrrev_b32_e32 v152, 3, v152\n"
      "s_add_u32 m0, s64, 1024\n"
      "v_add_u32 v124, 0x3800, v124\n"
      "global_load_lds_dwordx4 v124, s[70:71]\n"
      "v_lshlrev_b32_e32 v151, 1, v151\n"
      "s_or_b32 s4, s4, s5\n"
      "v_and_b32_e32 v151, 48, v151\n"
      "v_and_b32_e32 v152, 0xc0, v152\n"
      "v_lshl_or_b32 v145, v145, 2, s4\n"
      "s_lshl_b32 s2, s2, 5\n"
      "s_lshl_b32 s4, s57, 3\n"
      "v_or3_b32 v149, v149, v152, v151\n"
      "s_or_b32 s2, s2, s4\n"
      "v_or_b32_e32 v220, 0x7000, v146\n"
      "v_lshl_or_b32 v221, v149, 10, v146\n"
      "v_or_b32_e32 v210, 0x1400000, v145\n"
      "s_add_u32 m0, s64, 2048\n"
      "v_add_u32 v124, 0xa800, v124\n"
      "global_load_lds_dwordx4 v125, s[70:71]\n"
      "v_or_b32_e32 v145, s2, v148\n"
      "v_mov_b32_e32 v146, 0x1400000\n"
      "v_lshl_add_u32 v211, v145, 9, v146\n"
      "v_mov_b32_e32 v145, s1\n"
      "v_lshlrev_b32_e32 v147, 6, v223\n"
      "s_lshl_b32 s3, s57, 11\n"
      "v_bitop3_b32 v145, s0, v150, v145 bitop3:0x36\n"
      "s_lshl_b32 s0, s98, 13\n"
      "v_and_b32_e32 v144, 48, v223\n"
      "v_and_b32_e32 v147, 0x3c0, v147\n"
      "v_lshlrev_b32_e32 v145, 5, v145\n"
      "s_add_i32 s3, s3, s0\n"
      "s_add_u32 m0, s64, 3072\n"
      "v_add_u32 v125, 0x3800, v125\n"
      "global_load_lds_dwordx4 v125, s[70:71]\n"
      "v_or3_b32 v145, v145, s4, v148\n"
      "v_or3_b32 v144, v144, v147, s3\n"
      "v_add_u32_e32 v218, 0x683000, v153\n"
      "v_add_u32_e32 v219, 0x68b000, v153\n"
      "v_lshl_add_u32 v212, v145, 9, v146\n"
      "v_add_u32_e32 v213, 0x683000, v144\n"
      "s_and_b32 s0, s57, 1\n"
      "v_and_b32_e32 v144, 15, v223\n"
      "v_lshrrev_b32_e32 v145, 4, v223\n"
      "s_lshl_b32 s5, s0, 4\n"
      "v_mov_b32_e32 v148, 0x7800\n"
      "s_mul_i32 s1, s97, 14\n"
      "s_add_u32 m0, s64, 4096\n"
      "v_add_u32 v125, 0xa800, v125\n"
      "global_load_lds_dwordx4 v124, s[70:71]\n"
      "s_and_b32 s2, s97, 8\n"
      "s_lshl_b32 s3, s57, 1\n"
      "v_or_b32_e32 v147, s5, v144\n"
      "v_lshl_or_b32 v148, v145, 8, v148\n"
      "s_movk_i32 s6, 0x410\n"
      "v_lshrrev_b32_e32 v146, 5, v223\n"
      "s_or_b32 s2, s2, s3\n"
      "v_mad_u32_u24 v242, v147, s6, v148\n"
      "v_bitop3_b32 v147, s5, 16, v144 bitop3:0x36\n"
      "s_add_i32 s3, s1, s3\n"
      "v_mad_u32_u24 v243, v147, s6, v148\n"
      "v_or_b32_e32 v147, s3, v146\n"
      "s_add_u32 m0, s64, 5120\n"
      "v_add_u32 v124, 0x3800, v124\n"
      "global_load_lds_dwordx4 v124, s[70:71]\n"
      "v_xor_b32_e32 v145, s57, v145\n"
      "s_lshr_b32 s3, s57, 1\n"
      "v_lshlrev_b32_e32 v145, 8, v145\n"
      "s_add_i32 s3, s1, s3\n"
      "v_lshlrev_b32_e32 v147, 9, v147\n"
      "v_and_b32_e32 v145, 0x100, v145\n"
      "v_lshlrev_b32_e32 v144, 4, v144\n"
      "s_lshl_b32 s3, s3, 9\n"
      "s_lshl_b32 s0, s0, 8\n"
      "s_add_i32 s1, s1, s57\n"
      "v_or3_b32 v245, v147, v144, v145\n"
      "s_or_b32 s0, s3, s0\n"
      "s_add_u32 m0, s64, 6144\n"
      "v_add_u32 v124, 0x26800, v124\n"
      "global_load_lds_dwordx4 v125, s[70:71]\n"
      "v_lshl_or_b32 v145, s1, 9, v145\n"
      "s_addk_i32 s0, 0x1800\n"
      "v_add_u32_e32 v145, 0x1000, v145\n"
      "v_sub_u32_e32 v146, 0, v146\n"
      "v_cmp_gt_u32_e32 vcc, 32, v223\n"
      "s_mul_i32 s4, s2, 7\n"
      "v_and_b32_e32 v146, s0, v146\n"
      "v_cndmask_b32_e32 v145, 0, v145, vcc\n"
      "s_lshl_b32 s0, s97, 1\n"
      "v_or3_b32 v246, v146, v145, v144\n"
      "s_and_b32 s0, s0, 14\n"
      "v_mov_b32_e32 v144, 0x70\n"
      "s_add_u32 m0, s64, 7168\n"
      "v_add_u32 v125, 0x3800, v125\n"
      "global_load_lds_dwordx4 v125, s[70:71]\n"
      "v_mov_b32_e32 v145, s4\n"
      "v_lshlrev_b32_e32 v240, 4, v223\n"
      "v_mad_u32_u24 v145, s0, v144, v145\n"
      "v_lshl_add_u32 v145, v145, 10, v240\n"
      "s_movk_i32 s1, 0x70\n"
      "v_add_u32_e32 v247, 0xa85000, v145\n"
      "v_mov_b32_e32 v145, s0\n"
      "v_mad_u32_u24 v145, v145, s1, s1\n"
      "s_xor_b32 s2, s2, 8\n"
      "v_add_u32_e32 v146, s4, v145\n"
      "s_mul_i32 s2, s2, 7\n"
      "v_lshl_add_u32 v146, v146, 10, v240\n"
      "s_add_u32 m0, s64, 32768\n"
      "v_add_u32 v125, 0x26800, v125\n"
      "global_load_lds_dwordx4 v124, s[70:71]\n"
      "v_add_u32_e32 v248, 0xa85000, v146\n"
      "v_mov_b32_e32 v146, s2\n"
      "s_lshr_b32 s1, s97, 2\n"
      "v_mad_u32_u24 v144, s0, v144, v146\n"
      "s_lshl_b32 s0, s96, 2\n"
      "s_and_b32 s1, s1, 2\n"
      "v_lshl_add_u32 v144, v144, 10, v240\n"
      "s_or_b32 s0, s1, s0\n"
      "v_add_u32_e32 v249, 0xa85000, v144\n"
      "v_add_u32_e32 v144, s2, v145\n"
      "s_mulk_i32 s0, 0x70\n"
      "v_lshl_add_u32 v144, v144, 10, v240\n"
      "s_add_u32 m0, s64, 33792\n"
      "v_add_u32 v124, 0x3800, v124\n"
      "global_load_lds_dwordx4 v124, s[70:71]\n"
      "s_add_i32 s1, s0, s4\n"
      "v_add_u32_e32 v250, 0xa85000, v144\n"
      "v_lshl_add_u32 v144, s1, 10, v240\n"
      "s_add_i32 s1, s0, 0x70\n"
      "s_add_i32 s3, s1, s4\n"
      "s_mul_i32 s5, s57, 0x2080\n"
      "v_add_u32_e32 v251, 0xc86000, v144\n"
      "v_lshl_add_u32 v144, s3, 10, v240\n"
      "s_add_i32 s0, s0, s2\n"
      "s_addk_i32 s5, 0x7800\n"
      "v_add_u32_e32 v252, 0xc86000, v144\n"
      "v_lshl_add_u32 v144, s0, 10, v240\n"
      "s_lshl_b32 s0, s57, 7\n"
      "s_add_i32 s1, s1, s2\n"
      "s_sub_i32 s0, s5, s0\n"
      "v_add_u32_e32 v253, 0xc86000, v144\n"
      "v_lshl_add_u32 v144, s1, 10, v240\n"
      "v_or_b32_e32 v255, s0, v240\n"
      "s_mul_i32 s0, s57, 0x1c00\n"
      "v_or_b32_e32 v241, 0x1000, v240\n"
      "v_add_u32_e32 v244, s5, v240\n"
      "v_add_u32_e32 v254, 0xc86000, v144\n"
      "v_or_b32_e32 v222, s0, v240\n"
      "global_load_dwordx4 v[160:163], v232, s[52:53] offset:0 sc1\n"
      "global_load_dwordx4 v[164:167], v232, s[88:89] offset:0 sc1\n"
      "ds_read_b64_tr_b16 a[64:65], v236 offset:0\n"
      "ds_read_b64_tr_b16 a[66:67], v236 offset:1024\n"
      "ds_read_b64_tr_b16 a[68:69], v237 offset:0\n"
      "ds_read_b64_tr_b16 a[70:71], v237 offset:1024\n"
      "ds_read_b64_tr_b16 a[72:73], v238 offset:0\n"
      "ds_read_b64_tr_b16 a[74:75], v238 offset:1024\n"
      "ds_read_b64_tr_b16 a[76:77], v239 offset:0\n"
      "ds_read_b64_tr_b16 a[78:79], v239 offset:1024\n"
      "ds_read_b64_tr_b16 a[80:81], v236 offset:16384\n"
      "ds_read_b64_tr_b16 a[82:83], v236 offset:17408\n"
      "ds_read_b64_tr_b16 a[84:85], v237 offset:16384\n"
      "ds_read_b64_tr_b16 a[86:87], v237 offset:17408\n"
      "ds_read_b64_tr_b16 a[88:89], v238 offset:16384\n"
      "ds_read_b64_tr_b16 a[90:91], v238 offset:17408\n"
      "ds_read_b64_tr_b16 a[92:93], v239 offset:16384\n"
      "ds_read_b64_tr_b16 a[94:95], v239 offset:17408\n"
      "s_waitcnt vmcnt(17)\n"
      "v_lshlrev_b32 v184, 16, v128\n"
      "v_and_b32 v188, 0xffff0000, v128\n"
      "v_lshlrev_b32 v185, 16, v129\n"
      "v_and_b32 v189, 0xffff0000, v129\n"
      "v_lshlrev_b32 v186, 16, v130\n"
      "v_and_b32 v190, 0xffff0000, v130\n"
      "v_lshlrev_b32 v187, 16, v131\n"
      "v_and_b32 v191, 0xffff0000, v131\n"
      "v_pk_add_f32 v[184:185], v[184:185], s[78:79] op_sel_hi:[1,0]\n"
      "v_pk_add_f32 v[186:187], v[186:187], s[78:79] op_sel_hi:[1,0]\n"
      "v_pk_add_f32 v[188:189], v[188:189], s[78:79] op_sel_hi:[1,0]\n"
      "v_pk_add_f32 v[190:191], v[190:191], s[78:79] op_sel_hi:[1,0]\n"
      "s_waitcnt vmcnt(16)\n"
      "v_lshlrev_b32 v192, 16, v132\n"
      "v_and_b32 v196, 0xffff0000, v132\n"
      "v_lshlrev_b32 v193, 16, v133\n"
      "v_and_b32 v197, 0xffff0000, v133\n"
      "v_lshlrev_b32 v194, 16, v134\n"
      "v_and_b32 v198, 0xffff0000, v134\n"
      "v_lshlrev_b32 v195, 16, v135\n"
      "v_and_b32 v199, 0xffff0000, v135\n"
      "v_pk_add_f32 v[184:185], v[184:185], v[192:193]\n"
      "v_pk_add_f32 v[186:187], v[186:187], v[194:195]\n"
      "v_pk_add_f32 v[188:189], v[188:189], v[196:197]\n"
      "v_pk_add_f32 v[190:191], v[190:191], v[198:199]\n"
      "s_waitcnt vmcnt(15)\n"
      "v_lshlrev_b32 v192, 16, v136\n"
      "v_and_b32 v196, 0xffff0000, v136\n"
      "v_lshlrev_b32 v193, 16, v137\n"
      "v_and_b32 v197, 0xffff0000, v137\n"
      "v_lshlrev_b32 v194, 16, v138\n"
      "v_and_b32 v198, 0xffff0000, v138\n"
      "v_lshlrev_b32 v195, 16, v139\n"
      "v_and_b32 v199, 0xffff0000, v139\n"
      "v_pk_add_f32 v[184:185], v[184:185], v[192:193]\n"
      "v_pk_add_f32 v[186:187], v[186:187], v[194:195]\n"
      "v_pk_add_f32 v[188:189], v[188:189], v[196:197]\n"
      "v_pk_add_f32 v[190:191], v[190:191], v[198:199]\n"
      "s_waitcnt vmcnt(14)\n"
      "v_lshlrev_b32 v192, 16, v140\n"
      "v_and_b32 v196, 0xffff0000, v140\n"
      "v_lshlrev_b32 v193, 16, v141\n"
      "v_and_b32 v197, 0xffff0000, v141\n"
      "v_lshlrev_b32 v194, 16, v142\n"
      "v_and_b32 v198, 0xffff0000, v142\n"
      "v_lshlrev_b32_e64 v195, 16, v143\n"
      "v_and_b32 v199, 0xffff0000, v143\n"
      "v_pk_add_f32 v[184:185], v[184:185], v[192:193]\n"
      "v_pk_add_f32 v[186:187], v[186:187], v[194:195]\n"
      "v_pk_add_f32 v[188:189], v[188:189], v[196:197]\n"
      "v_pk_add_f32 v[190:191], v[190:191], v[198:199]\n"
      "ds_write_b128 v209, v[184:187]\n"
      "ds_write_b128 v209, v[188:191] offset:1024\n"
      "s_mov_b32 s76, 0x3b000000\n"
      "s_mov_b32 s77, 0x3727c5ac\n"
      "s_waitcnt vmcnt(4)\n"
      "s_waitcnt lgkmcnt(0)\n"
      "s_barrier\n"
      "ds_read_b64_tr_b16 a[96:97], v236 offset:32768\n"
      "ds_read_b64_tr_b16 a[98:99], v236 offset:33792\n"
      "ds_read_b64_tr_b16 a[100:101], v237 offset:32768\n"
      "ds_read_b64_tr_b16 a[102:103], v237 offset:33792\n"
      "ds_read_b64_tr_b16 a[104:105], v238 offset:32768\n"
      "ds_read_b64_tr_b16 a[106:107], v238 offset:33792\n"
      "ds_read_b64_tr_b16 a[108:109], v239 offset:32768\n"
      "ds_read_b64_tr_b16 a[110:111], v239 offset:33792\n"
      "ds_read_b64_tr_b16 a[112:113], v236 offset:49152\n"
      "ds_read_b64_tr_b16 a[114:115], v236 offset:50176\n"
      "ds_read_b64_tr_b16 a[116:117], v237 offset:49152\n"
      "ds_read_b64_tr_b16 a[118:119], v237 offset:50176\n"
      "ds_read_b64_tr_b16 a[120:121], v238 offset:49152\n"
      "ds_read_b64_tr_b16 a[122:123], v238 offset:50176\n"
      "ds_read_b64_tr_b16 a[124:125], v239 offset:49152\n"
      "ds_read_b64_tr_b16 a[126:127], v239 offset:50176\n"
      "v_xor_b32 v236, 0x10000, v236\n"
      "v_xor_b32 v237, 0x10000, v237\n"
      "v_xor_b32 v238, 0x10000, v238\n"
      "v_xor_b32 v239, 0x10000, v239\n"
      "ds_read_b64_tr_b16 a[128:129], v236 offset:0\n"
      "ds_read_b64_tr_b16 a[130:131], v236 offset:1024\n"
      "ds_read_b64_tr_b16 a[132:133], v237 offset:0\n"
      "ds_read_b64_tr_b16 a[134:135], v237 offset:1024\n"
      "ds_read_b64_tr_b16 a[136:137], v238 offset:0\n"
      "ds_read_b64_tr_b16 a[138:139], v238 offset:1024\n"
      "ds_read_b64_tr_b16 a[140:141], v239 offset:0\n"
      "ds_read_b64_tr_b16 a[142:143], v239 offset:1024\n"
      "ds_read_b64_tr_b16 a[144:145], v236 offset:16384\n"
      "ds_read_b64_tr_b16 a[146:147], v236 offset:17408\n"
      "ds_read_b64_tr_b16 a[148:149], v237 offset:16384\n"
      "ds_read_b64_tr_b16 a[150:151], v237 offset:17408\n"
      "ds_read_b64_tr_b16 a[152:153], v238 offset:16384\n"
      "ds_read_b64_tr_b16 a[154:155], v238 offset:17408\n"
      "ds_read_b64_tr_b16 a[156:157], v239 offset:16384\n"
      "ds_read_b64_tr_b16 a[158:159], v239 offset:17408\n"
      "s_branch .Lq_Lsk_v\n"
      ".Lq_Rv:\n"
      "s_sub_u32 s69, s69, 1\n"
      "s_cbranch_scc1 .Lq_Tv\n"
      "s_mov_b64 exec, vcc\n"
      "global_load_dwordx4 v[160:163], v232, s[52:53] offset:0 sc1\n"
      "global_load_dwordx4 v[164:167], v232, s[88:89] offset:0 sc1\n"
      "s_mov_b64 exec, -1\n"
      "s_waitcnt vmcnt(0)\n"
      "v_max3_u32 v144, v160, v161, v162\n"
      "v_max3_u32 v145, v163, v164, v165\n"
      "v_max3_u32 v144, v144, v166, v167\n"
      "v_max_u32 v144, v144, v145\n"
      "v_cmp_eq_u32 vcc, -1, v144\n"
      "s_cbranch_vccnz .Lq_Rv\n"
      "s_branch .Lq_Cv\n"
      ".Lq_Tv:\n"
      "s_mov_b32 s69, 0\n"
      "s_branch .Lq_Cv\n"
      ".Lq_Lsk_v:\n"
      "s_waitcnt vmcnt(1)\n"
      "v_max3_u32 v144, v160, v161, v162\n"
      "s_waitcnt vmcnt(0)\n"
      "v_max3_u32 v145, v163, v164, v165\n"
      "v_max3_u32 v144, v144, v166, v167\n"
      "v_max_u32 v144, v144, v145\n"
      "v_cmp_eq_u32 vcc, -1, v144\n"
      "s_cbranch_vccnz .Lq_Rv\n"
      ".Lq_Cv:\n"
      "v_mfma_f32_32x32x16_bf16 v[128:143], v[72:75], v[160:163], 0\n"
      "v_mfma_f32_32x32x16_bf16 v[144:159], v[72:75], v[164:167], 0\n"
      "s_add_u32 m0, s64, 34816\n"
      "v_add_u32 v124, 0xa800, v124\n"
      "global_load_lds_dwordx4 v125, s[70:71]\n"
      "s_nop 0\n"
      "s_nop 6\n"
      "v_cvt_pk_bf16_f32 v160, v128, v129\n"
      "v_cvt_pk_bf16_f32 v161, v130, v131\n"
      "v_cvt_pk_bf16_f32 v162, v132, v133\n"
      "v_cvt_pk_bf16_f32 v163, v134, v135\n"
      "ds_write2st64_b32 v226, v160, v161 offset0:0 offset1:5\n"
      "ds_write2st64_b32 v226, v162, v163 offset0:10 offset1:15\n"
      "v_cvt_pk_bf16_f32 v164, v144, v145\n"
      "v_cvt_pk_bf16_f32 v165, v146, v147\n"
      "v_cvt_pk_bf16_f32 v166, v148, v149\n"
      "v_cvt_pk_bf16_f32 v167, v150, v151\n"
      "ds_write2st64_b32 v227, v164, v165 offset0:0 offset1:5\n"
      "ds_write2st64_b32 v227, v166, v167 offset0:10 offset1:15\n"
      "s_add_u32 m0, s64, 35840\n"
      "v_add_u32 v125, 0x3800, v125\n"
      "global_load_lds_dwordx4 v125, s[70:71]\n"
      "s_waitcnt lgkmcnt(0)\n"
      "s_barrier\n"
      "ds_read_b128 v[152:155], v225\n"
      "ds_read_b128 v[156:159], v225 offset:1280\n"
      "ds_read_b128 v[184:187], v235 offset:0\n"
      "ds_read_b128 v[188:191], v235 offset:1024\n"
      "ds_read_b128 v[192:195], v235 offset:4096\n"
      "ds_read_b128 v[196:199], v235 offset:5120\n"
      "ds_read_b128 v[200:203], v235 offset:2048\n"
      "ds_read_b128 v[204:207], v235 offset:3072\n"
      "s_add_u32 m0, s64, 36864\n"
      "v_add_u32 v125, 0xa800, v125\n"
      "global_load_lds_dwordx4 v124, s[70:71]\n"
      "s_waitcnt lgkmcnt(7)\n"
      "v_mfma_f32_16x16x32_bf16 v[128:131], v[152:155], v[76:79], 0\n"
      "v_mfma_f32_16x16x32_bf16 v[132:135], v[152:155], v[80:83], 0\n"
      "s_waitcnt lgkmcnt(6)\n"
      "v_mfma_f32_16x16x32_bf16 v[136:139], v[156:159], v[76:79], 0\n"
      "v_mfma_f32_16x16x32_bf16 v[140:143], v[156:159], v[80:83], 0\n"
      "s_add_u32 m0, s64, 37888\n"
      "v_add_u32 v124, 0x3800, v124\n"
      "global_load_lds_dwordx4 v124, s[70:71]\n"
      "s_nop 0\n"
      "s_nop 0\n"
      "v_pk_mul_f32 v[144:145], v[128:129], v[20:21]\n"
      "v_pk_mul_f32 v[148:149], v[132:133], v[20:21]\n"
      "v_pk_mul_f32 v[146:147], v[130:131], v[22:23]\n"
      "v_pk_mul_f32 v[150:151], v[134:135], v[22:23]\n"
      "v_pk_fma_f32 v[144:145], v[132:133], v[24:25], v[144:145]  neg_lo:[1,0,0] neg_hi:[1,0,0]\n"
      "v_pk_fma_f32 v[148:149], v[128:129], v[24:25], v[148:149]\n"
      "v_pk_fma_f32 v[146:147], v[134:135], v[26:27], v[146:147]  neg_lo:[1,0,0] neg_hi:[1,0,0]\n"
      "v_pk_fma_f32 v[150:151], v[130:131], v[26:27], v[150:151]\n"
      "v_pk_mul_f32 v[160:161], v[136:137], v[20:21]\n"
      "v_pk_mul_f32 v[164:165], v[140:141], v[20:21]\n"
      "v_pk_mul_f32 v[162:163], v[138:139], v[22:23]\n"
      "v_pk_mul_f32 v[166:167], v[142:143], v[22:23]\n"
      "v_pk_fma_f32 v[160:161], v[140:141], v[24:25], v[160:161]  neg_lo:[1,0,0] neg_hi:[1,0,0]\n"
      "v_pk_fma_f32 v[164:165], v[136:137], v[24:25], v[164:165]\n"
      "v_pk_fma_f32 v[162:163], v[142:143], v[26:27], v[162:163]  neg_lo:[1,0,0] neg_hi:[1,0,0]\n"
      "v_pk_fma_f32 v[166:167], v[138:139], v[26:27], v[166:167]\n"
      "v_cvt_pk_bf16_f32 v152, v144, v148\n"
      "v_cvt_pk_bf16_f32 v153, v145, v149\n"
      "v_cvt_pk_bf16_f32 v154, v146, v150\n"
      "v_cvt_pk_bf16_f32 v155, v147, v151\n"
      "v_cvt_pk_bf16_f32 v156, v160, v164\n"
      "v_cvt_pk_bf16_f32 v157, v161, v165\n"
      "v_mfma_f32_16x16x32_bf16 v[128:131], v[152:155], v[48:51], 0\n"
      "v_cvt_pk_bf16_f32 v158, v162, v166\n"
      "v_cvt_pk_bf16_f32 v159, v163, v167\n"
      "v_mfma_f32_16x16x32_bf16 v[132:135], v[152:155], v[52:55], 0\n"
      "s_nop 0\n"
      "v_mfma_f32_16x16x32_bf16 v[136:139], v[156:159], v[48:51], 0\n"
      "v_mfma_f32_16x16x32_bf16 v[140:143], v[156:159], v[52:55], 0\n"
      "s_add_u32 m0, s64, 38912\n"
      "v_add_u32 v124, 0x26800, v124\n"
      "global_load_lds_dwordx4 v125, s[70:71]\n"
      "s_waitcnt lgkmcnt(5)\n"
      "v_pk_fma_f32 v[168:169], v[184:185], v[128:129], v[168:169]\n"
      "v_pk_fma_f32 v[170:171], v[186:187], v[130:131], v[170:171]\n"
      "s_waitcnt vmcnt(63) expcnt(7) lgkmcnt(15)\n"
      "s_waitcnt lgkmcnt(4)\n"
      "v_pk_fma_f32 v[172:173], v[188:189], v[132:133], v[172:173]\n"
      "v_pk_fma_f32 v[174:175], v[190:191], v[134:135], v[174:175]\n"
      "v_pk_add_f32 v[144:145], v[168:169], v[170:171]\n"
      "v_pk_mul_f32 v[146:147], v[168:169], v[168:169]\n"
      "v_pk_add_f32 v[144:145], v[144:145], v[172:173]\n"
      "v_pk_fma_f32 v[146:147], v[170:171], v[170:171], v[146:147]\n"
      "v_pk_add_f32 v[144:145], v[144:145], v[174:175]\n"
      "v_pk_fma_f32 v[146:147], v[172:173], v[172:173], v[146:147]\n"
      "v_add_f32 v152, v144, v145\n"
      "v_pk_fma_f32 v[146:147], v[174:175], v[174:175], v[146:147]\n"
      "v_add_f32 v154, v146, v147\n"
      "v_pk_fma_f32 v[176:177], v[184:185], v[136:137], v[176:177]\n"
      "v_pk_fma_f32 v[178:179], v[186:187], v[138:139], v[178:179]\n"
      "v_pk_fma_f32 v[180:181], v[188:189], v[140:141], v[180:181]\n"
      "v_pk_fma_f32 v[182:183], v[190:191], v[142:143], v[182:183]\n"
      "v_pk_add_f32 v[148:149], v[176:177], v[178:179]\n"
      "v_pk_mul_f32 v[150:151], v[176:177], v[176:177]\n"
      "v_pk_add_f32 v[148:149], v[148:149], v[180:181]\n"
      "v_pk_fma_f32 v[150:151], v[178:179], v[178:179], v[150:151]\n"
      "v_pk_add_f32 v[148:149], v[148:149], v[182:183]\n"
      "s_add_u32 m0, s64, 39936\n"
      "v_add_u32 v125, 0x3800, v125\n"
      "global_load_lds_dwordx4 v125, s[70:71]\n"
      "v_pk_fma_f32 v[150:151], v[180:181], v[180:181], v[150:151]\n"
      "v_add_f32 v153, v148, v149\n"
      "v_pk_fma_f32 v[150:151], v[182:183], v[182:183], v[150:151]\n"
      "v_add_f32 v155, v150, v151\n"
      "v_add_f32_dpp v152, v152, v152 quad_perm:[1,0,3,2] row_mask:0xf bank_mask:0xf\n"
      "v_add_f32_dpp v154, v154, v154 quad_perm:[1,0,3,2] row_mask:0xf bank_mask:0xf\n"
      "v_add_f32_dpp v153, v153, v153 quad_perm:[1,0,3,2] row_mask:0xf bank_mask:0xf\n"
      "v_add_f32_dpp v155, v155, v155 quad_perm:[1,0,3,2] row_mask:0xf bank_mask:0xf\n"
      "v_add_f32_dpp v152, v152, v152 quad_perm:[2,3,0,1] row_mask:0xf bank_mask:0xf\n"
      "v_add_f32_dpp v154, v154, v154 quad_perm:[2,3,0,1] row_mask:0xf bank_mask:0xf\n"
      "v_add_f32_dpp v153, v153, v153 quad_perm:[2,3,0,1] row_mask:0xf bank_mask:0xf\n"
      "v_add_f32_dpp v155, v155, v155 quad_perm:[2,3,0,1] row_mask:0xf bank_mask:0xf\n"
      "v_add_f32_dpp v152, v152, v152 row_half_mirror row_mask:0xf bank_mask:0xf\n"
      "v_add_f32_dpp v154, v154, v154 row_half_mirror row_mask:0xf bank_mask:0xf\n"
      "v_add_f32_dpp v153, v153, v153 row_half_mirror row_mask:0xf bank_mask:0xf\n"
      "v_add_f32_dpp v155, v155, v155 row_half_mirror row_mask:0xf bank_mask:0xf\n"
      "v_add_f32_dpp v152, v152, v152 row_mirror row_mask:0xf bank_mask:0xf\n"
      "v_add_f32_dpp v154, v154, v154 row_mirror row_mask:0xf bank_mask:0xf\n"
      "v_add_f32_dpp v153, v153, v153 row_mirror row_mask:0xf bank_mask:0xf\n"
      "v_add_f32_dpp v155, v155, v155 row_mirror row_mask:0xf bank_mask:0xf\n"
      "v_add_f32_dpp v152, v152, v152 row_bcast:15 row_mask:0xa bank_mask:0xf\n"
      "v_add_f32_dpp v154, v154, v154 row_bcast:15 row_mask:0xa bank_mask:0xf\n"
      "s_add_u32 m0, s64, 65536\n"
      "v_add_u32 v125, 0x26800, v125\n"
      "global_load_lds_dwordx4 v124, s[70:71]\n"
      "v_add_f32_dpp v153, v153, v153 row_bcast:15 row_mask:0xa bank_mask:0xf\n"
      "v_add_f32_dpp v155, v155, v155 row_bcast:15 row_mask:0xa bank_mask:0xf\n"
      "v_add_f32_dpp v152, v152, v152 row_bcast:31 row_mask:0xc bank_mask:0xf\n"
      "v_add_f32_dpp v154, v154, v154 row_bcast:31 row_mask:0xc bank_mask:0xf\n"
      "v_add_f32_dpp v153, v153, v153 row_bcast:31 row_mask:0xc bank_mask:0xf\n"
      "v_add_f32_dpp v155, v155, v155 row_bcast:31 row_mask:0xc bank_mask:0xf\n"
      "v_pk_mul_f32 v[152:153], v[152:153], s[76:77] op_sel:[0,0] op_sel_hi:[1,0]\n"
      "v_pk_mul_f32 v[154:155], v[154:155], s[76:77] op_sel:[0,0] op_sel_hi:[1,0]\n"
      "v_pk_fma_f32 v[154:155], v[152:153], v[152:153], v[154:155]  neg_lo:[1,0,0] neg_hi:[1,0,0]\n"
      "v_max_f32 v154, 0, v154\n"
      "v_max_f32 v155, 0, v155\n"
      "v_pk_add_f32 v[154:155], v[154:155], s[76:77] op_sel:[0,1] op_sel_hi:[1,1]\n"
      "v_rsq_f32 v154, v154\n"
      "v_rsq_f32 v155, v155\n"
      "s_nop 0\n"
      "v_pk_mul_f32 v[152:153], v[152:153], v[154:155] neg_lo:[1,0] neg_hi:[1,0]\n"
      "v_readlane_b32 s76, v154, 63\n"
      "v_readlane_b32 s78, v155, 63\n"
      "v_readlane_b32 s77, v152, 63\n"
      "v_readlane_b32 s79, v153, 63\n"
      "s_nop 0\n"
      "v_pk_fma_f32 v[128:129], v[168:169], s[76:77], s[76:77] op_sel:[0,0,1] op_sel_hi:[1,0,1]\n"
      "v_pk_fma_f32 v[130:131], v[170:171], s[76:77], s[76:77] op_sel:[0,0,1] op_sel_hi:[1,0,1]\n"
      "v_pk_fma_f32 v[132:133], v[172:173], s[76:77], s[76:77] op_sel:[0,0,1] op_sel_hi:[1,0,1]\n"
      "s_add_u32 m0, s64, 66560\n"
      "v_add_u32 v124, 0x3800, v124\n"
      "global_load_lds_dwordx4 v124, s[70:71]\n"
      "v_pk_fma_f32 v[134:135], v[174:175], s[76:77], s[76:77] op_sel:[0,0,1] op_sel_hi:[1,0,1]\n"
      "s_waitcnt lgkmcnt(1)\n"
      "v_pk_fma_f32 v[128:129], v[128:129], v[192:193], v[200:201]\n"
      "v_pk_fma_f32 v[130:131], v[130:131], v[194:195], v[202:203]\n"
      "s_waitcnt lgkmcnt(0)\n"
      "v_pk_fma_f32 v[132:133], v[132:133], v[196:197], v[204:205]\n"
      "v_pk_fma_f32 v[134:135], v[134:135], v[198:199], v[206:207]\n"
      "v_cvt_pk_bf16_f32 v160, v128, v132\n"
      "v_cvt_pk_bf16_f32 v161, v129, v133\n"
      "v_cvt_pk_bf16_f32 v162, v130, v134\n"
      "v_cvt_pk_bf16_f32 v163, v131, v135\n"
      "s_add_u32 m0, s64, 67584\n"
      "v_add_u32 v124, 0xa800, v124\n"
      "global_load_lds_dwordx4 v125, s[70:71]\n"
      "global_store_dwordx4 v233, v[160:163], s[52:53] offset:0 sc1\n"
      "v_pk_fma_f32 v[136:137], v[176:177], s[78:79], s[78:79] op_sel:[0,0,1] op_sel_hi:[1,0,1]\n"
      "v_pk_fma_f32 v[138:139], v[178:179], s[78:79], s[78:79] op_sel:[0,0,1] op_sel_hi:[1,0,1]\n"
      "v_pk_fma_f32 v[140:141], v[180:181], s[78:79], s[78:79] op_sel:[0,0,1] op_sel_hi:[1,0,1]\n"
      "v_pk_fma_f32 v[142:143], v[182:183], s[78:79], s[78:79] op_sel:[0,0,1] op_sel_hi:[1,0,1]\n"
      "v_pk_fma_f32 v[136:137], v[136:137], v[192:193], v[200:201]\n"
      "v_pk_fma_f32 v[138:139], v[138:139], v[194:195], v[202:203]\n"
      "v_pk_fma_f32 v[140:141], v[140:141], v[196:197], v[204:205]\n"
      "v_pk_fma_f32 v[142:143], v[142:143], v[198:199], v[206:207]\n"
      "v_cvt_pk_bf16_f32 v164, v136, v140\n"
      "v_cvt_pk_bf16_f32 v165, v137, v141\n"
      "v_cvt_pk_bf16_f32 v166, v138, v142\n"
      "v_cvt_pk_bf16_f32 v167, v139, v143\n"
      "global_store_dwordx4 v233, v[164:167], s[52:53] offset:1024 sc1\n"
      "v_cvt_pk_bf16_f32 v152, v168, v172\n"
      "v_cvt_pk_bf16_f32 v153, v169, v173\n"
      "v_cvt_pk_bf16_f32 v154, v170, v174\n"
      "v_cvt_pk_bf16_f32 v155, v171, v175\n"
      "v_cvt_pk_bf16_f32 v156, v176, v180\n"
      "v_cvt_pk_bf16_f32 v157, v177, v181\n"
      "v_cvt_pk_bf16_f32 v158, v178, v182\n"
      "v_cvt_pk_bf16_f32 v159, v179, v183\n"
      "s_add_u32 m0, s64, 68608\n"
      "v_add_u32 v125, 0x3800, v125\n"
      "global_load_lds_dwordx4 v125, s[70:71]\n"
      "s_add_u32 m0, s64, 69632\n"
      "v_add_u32 v125, 0xa800, v125\n"
      "global_load_lds_dwordx4 v124, s[70:71]\n"
      "s_add_u32 m0, s64, 70656\n"
      "v_add_u32 v124, 0x3800, v124\n"
      "global_load_lds_dwordx4 v124, s[70:71]\n"
      "s_add_u32 m0, s64, 71680\n"
      "v_add_u32 v124, 0x26800, v124\n"
      "global_load_lds_dwordx4 v125, s[70:71]\n"
      "s_add_u32 m0, s64, 72704\n"
      "v_add_u32 v125, 0x3800, v125\n"
      "global_load_lds_dwordx4 v125, s[70:71]\n"
      "s_waitcnt vmcnt(10)\n"
      "s_barrier\n"
      "ds_read_b64_tr_b16 a[160:161], v236 offset:32768\n"
      "ds_read_b64_tr_b16 a[162:163], v236 offset:33792\n"
      "ds_read_b64_tr_b16 a[164:165], v237 offset:32768\n"
      "ds_read_b64_tr_b16 a[166:167], v237 offset:33792\n"
      "ds_read_b64_tr_b16 a[168:169], v238 offset:32768\n"
      "ds_read_b64_tr_b16 a[170:171], v238 offset:33792\n"
      "ds_read_b64_tr_b16 a[172:173], v239 offset:32768\n"
      "ds_read_b64_tr_b16 a[174:175], v239 offset:33792\n"
      "ds_read_b64_tr_b16 a[176:177], v236 offset:49152\n"
      "ds_read_b64_tr_b16 a[178:179], v236 offset:50176\n"
      "ds_read_b64_tr_b16 a[180:181], v237 offset:49152\n"
      "ds_read_b64_tr_b16 a[182:183], v237 offset:50176\n"
      "ds_read_b64_tr_b16 a[184:185], v238 offset:49152\n"
      "ds_read_b64_tr_b16 a[186:187], v238 offset:50176\n"
      "ds_read_b64_tr_b16 a[188:189], v239 offset:49152\n"
      "ds_read_b64_tr_b16 a[190:191], v239 offset:50176\n"
      "v_xor_b32 v236, 0x10000, v236\n"
      "v_xor_b32 v237, 0x10000, v237\n"
      "v_xor_b32 v238, 0x10000, v238\n"
      "v_xor_b32 v239, 0x10000, v239\n"
      "s_waitcnt lgkmcnt(0)\n"
      "s_barrier\n"
      "s_add_u32 m0, s64, 98304\n"
      "v_add_u32 v125, 0x26800, v125\n"
      "global_load_lds_dwordx4 v124, s[70:71]\n"
      "s_add_u32 m0, s64, 99328\n"
      "v_add_u32 v124, 0x3800, v124\n"
      "global_load_lds_dwordx4 v124, s[70:71]\n"
      "s_add_u32 m0, s64, 100352\n"
      "v_add_u32 v124, 0xa800, v124\n"
      "global_load_lds_dwordx4 v125, s[70:71]\n"
      "s_add_u32 m0, s64, 101376\n"
      "v_add_u32 v125, 0x3800, v125\n"
      "global_load_lds_dwordx4 v125, s[70:71]\n"
      "s_add_u32 m0, s64, 102400\n"
      "v_add_u32 v125, 0xa800, v125\n"
      "global_load_lds_dwordx4 v124, s[70:71]\n"
      "s_add_u32 m0, s64, 103424\n"
      "v_add_u32 v124, 0x3800, v124\n"
      "global_load_lds_dwordx4 v124, s[70:71]\n"
      "s_add_u32 m0, s64, 104448\n"
      "v_add_u32 v124, 0x26800, v124\n"
      "global_load_lds_dwordx4 v125, s[70:71]\n"
      "s_add_u32 m0, s64, 105472\n"
      "v_add_u32 v125, 0x3800, v125\n"
      "global_load_lds_dwordx4 v125, s[70:71]\n"
      "s_waitcnt vmcnt(13)\n"
      "s_add_u32 m0, s61, 0\n"
      "s_nop 0\n"
      "global_load_lds_dwordx4 v240, s[16:17] offset:0 sc1\n"
      "s_add_u32 m0, s61, 16\n"
      "s_nop 0\n"
      "global_load_lds_dwordx4 v240, s[16:17] offset:1024 sc1\n"
      "s_add_u32 m0, s61, 32\n"
      "s_nop 0\n"
      "global_load_lds_dwordx4 v240, s[16:17] offset:2048 sc1\n"
      "s_add_u32 m0, s61, 48\n"
      "s_nop 0\n"
      "global_load_lds_dwordx4 v240, s[16:17] offset:3072 sc1\n"
      "s_add_u32 m0, s61, 4160\n"
      "s_nop 0\n"
      "global_load_lds_dwordx4 v241, s[16:17] offset:0 sc1\n"
      "s_add_u32 m0, s61, 4176\n"
      "s_nop 0\n"
      "global_load_lds_dwordx4 v241, s[16:17] offset:1024 sc1\n"
      "s_add_u32 m0, s61, 4192\n"
      "s_nop 0\n"
      "global_load_lds_dwordx4 v241, s[16:17] offset:2048 sc1\n"
      "s_add_u32 m0, s61, 4208\n"
      "s_nop 0\n"
      "global_load_lds_dwordx4 v241, s[16:17] offset:3072 sc1\n"
      "s_add_u32 m0, s61, 33280\n"
      "s_nop 0\n"
      "global_load_lds_dwordx4 v240, s[18:19] offset:0 sc1\n"
      "s_add_u32 m0, s61, 33296\n"
      "s_nop 0\n"
      "global_load_lds_dwordx4 v240, s[18:19] offset:1024 sc1\n"
      "s_add_u32 m0, s61, 33312\n"
      "s_nop 0\n"
      "global_load_lds_dwordx4 v240, s[18:19] offset:2048 sc1\n"
      "s_add_u32 m0, s61, 33328\n"
      "s_nop 0\n"
      "global_load_lds_dwordx4 v240, s[18:19] offset:3072 sc1\n"
      "s_add_u32 m0, s61, 37440\n"
      "s_nop 0\n"
      "global_load_lds_dwordx4 v241, s[18:19] offset:0 sc1\n"
      "s_add_u32 m0, s61, 37456\n"
      "s_nop 0\n"
      "global_load_lds_dwordx4 v241, s[18:19] offset:1024 sc1\n"
      "s_add_u32 m0, s61, 37472\n"
      "s_nop 0\n"
      "global_load_lds_dwordx4 v241, s[18:19] offset:2048 sc1\n"
      "s_add_u32 m0, s61, 37488\n"
      "s_nop 0\n"
      "global_load_lds_dwordx4 v241, s[18:19] offset:3072 sc1\n"
      "v_mov_b32 v108, -1\n"
      "v_mov_b32 v109, -1\n"
      "v_mov_b32 v110, -1\n"
      "v_mov_b32 v111, -1\n"
      "s_waitcnt vmcnt(16)\n"
      "s_barrier\n"
      "ds_read_b64_tr_b16 a[192:193], v236 offset:0\n"
      "ds_read_b64_tr_b16 a[194:195], v236 offset:1024\n"
      "ds_read_b64_tr_b16 a[196:197], v237 offset:0\n"
      "ds_read_b64_tr_b16 a[198:199], v237 offset:1024\n"
      "ds_read_b64_tr_b16 a[200:201], v238 offset:0\n"
      "ds_read_b64_tr_b16 a[202:203], v238 offset:1024\n"
      "ds_read_b64_tr_b16 a[204:205], v239 offset:0\n"
      "ds_read_b64_tr_b16 a[206:207], v239 offset:1024\n"
      "ds_read_b64_tr_b16 a[208:209], v236 offset:16384\n"
      "ds_read_b64_tr_b16 a[210:211], v236 offset:17408\n"
      "ds_read_b64_tr_b16 a[212:213], v237 offset:16384\n"
      "ds_read_b64_tr_b16 a[214:215], v237 offset:17408\n"
      "ds_read_b64_tr_b16 a[216:217], v238 offset:16384\n"
      "ds_read_b64_tr_b16 a[218:219], v238 offset:17408\n"
      "ds_read_b64_tr_b16 a[220:221], v239 offset:16384\n"
      "ds_read_b64_tr_b16 a[222:223], v239 offset:17408\n"
      "ds_read_b64_tr_b16 a[224:225], v236 offset:32768\n"
      "ds_read_b64_tr_b16 a[226:227], v236 offset:33792\n"
      "ds_read_b64_tr_b16 a[228:229], v237 offset:32768\n"
      "ds_read_b64_tr_b16 a[230:231], v237 offset:33792\n"
      "ds_read_b64_tr_b16 a[232:233], v238 offset:32768\n"
      "ds_read_b64_tr_b16 a[234:235], v238 offset:33792\n"
      "ds_read_b64_tr_b16 a[236:237], v239 offset:32768\n"
      "ds_read_b64_tr_b16 a[238:239], v239 offset:33792\n"
      "ds_read_b64_tr_b16 a[240:241], v236 offset:49152\n"
      "ds_read_b64_tr_b16 a[242:243], v236 offset:50176\n"
      "ds_read_b64_tr_b16 a[244:245], v237 offset:49152\n"
      "ds_read_b64_tr_b16 a[246:247], v237 offset:50176\n"
      "ds_read_b64_tr_b16 a[248:249], v238 offset:49152\n"
      "ds_read_b64_tr_b16 a[250:251], v238 offset:50176\n"
      "ds_read_b64_tr_b16 a[252:253], v239 offset:49152\n"
      "ds_read_b64_tr_b16 a[254:255], v239 offset:50176\n"
      "s_branch .Lq_Lsk_h\n"
      ".Lq_Rh0:\n"
      "s_sub_u32 s69, s69, 1\n"
      "s_cbranch_scc1 .Lq_Th0\n"
      "s_nop 7\n"
      "s_waitcnt lgkmcnt(0)\n"
      "s_add_u32 m0, s61, 0\n"
      "s_nop 0\n"
      "global_load_lds_dwordx4 v240, s[16:17] offset:0 sc1\n"
      "s_add_u32 m0, s61, 16\n"
      "s_nop 0\n"
      "global_load_lds_dwordx4 v240, s[16:17] offset:1024 sc1\n"
      "s_add_u32 m0, s61, 32\n"
      "s_nop 0\n"
      "global_load_lds_dwordx4 v240, s[16:17] offset:2048 sc1\n"
      "s_add_u32 m0, s61, 48\n"
      "s_nop 0\n"
      "global_load_lds_dwordx4 v240, s[16:17] offset:3072 sc1\n"
      "s_add_u32 m0, s61, 4160\n"
      "s_nop 0\n"
      "global_load_lds_dwordx4 v241, s[16:17] offset:0 sc1\n"
      "s_add_u32 m0, s61, 4176\n"
      "s_nop 0\n"
      "global_load_lds_dwordx4 v241, s[16:17] offset:1024 sc1\n"
      "s_add_u32 m0, s61, 4192\n"
      "s_nop 0\n"
      "global_load_lds_dwordx4 v241, s[16:17] offset:2048 sc1\n"
      "s_add_u32 m0, s61, 4208\n"
      "s_nop 0\n"
      "global_load_lds_dwordx4 v241, s[16:17] offset:3072 sc1\n"
      "s_waitcnt vmcnt(0)\n"
      "ds_read_b128 v[80:83], v244 offset:0\n"
      "ds_read_b128 v[84:87], v244 offset:1040\n"
      "ds_read_b128 v[88:91], v244 offset:2080\n"
      "ds_read_b128 v[92:95], v244 offset:3120\n"
      "s_waitcnt lgkmcnt(3)\n"
      "v_max3_u32 v200, v80, v81, v82\n"
      "v_mov_b32 v201, v83\n"
      "s_waitcnt lgkmcnt(2)\n"
      "v_max3_u32 v200, v200, v84, v85\n"
      "v_max3_u32 v201, v201, v86, v87\n"
      "s_waitcnt lgkmcnt(1)\n"
      "v_max3_u32 v200, v200, v88, v89\n"
      "v_max3_u32 v201, v201, v90, v91\n"
      "s_waitcnt lgkmcnt(0)\n"
      "v_max3_u32 v200, v200, v92, v93\n"
      "v_max3_u32 v201, v201, v94, v95\n"
      "ds_read_b128 v[80:83], v244 offset:4160\n"
      "ds_read_b128 v[84:87], v244 offset:5200\n"
      "ds_read_b128 v[88:91], v244 offset:6240\n"
      "ds_read_b128 v[92:95], v244 offset:7280\n"
      "s_waitcnt lgkmcnt(3)\n"
      "v_max3_u32 v200, v200, v80, v81\n"
      "v_max3_u32 v201, v201, v82, v83\n"
      "s_waitcnt lgkmcnt(2)\n"
      "v_max3_u32 v200, v200, v84, v85\n"
      "v_max3_u32 v201, v201, v86, v87\n"
      "s_waitcnt lgkmcnt(1)\n"
      "v_max3_u32 v200, v200, v88, v89\n"
      "v_max3_u32 v201, v201, v90, v91\n"
      "s_waitcnt lgkmcnt(0)\n"
      "v_max3_u32 v200, v200, v92, v93\n"
      "v_max3_u32 v201, v201, v94, v95\n"
      "v_max_u32 v200, v200, v201\n"
      "v_cmp_eq_u32 vcc, -1, v200\n"
      "s_cbranch_vccnz .Lq_Rh0\n"
      "s_branch .Lq_Ch0\n"
      ".Lq_Th0:\n"
      "s_mov_b32 s69, 0\n"
      "s_branch .Lq_Ch0\n"
      ".Lq_Lsk_h:\n"
      "s_waitcnt vmcnt(63) expcnt(7) lgkmcnt(15)\n"
      "s_waitcnt vmcnt(8)\n"
      "ds_read_b128 v[0:3], v244 offset:0\n"
      "ds_read_b128 v[4:7], v244 offset:1040\n"
      "ds_read_b128 v[8:11], v244 offset:2080\n"
      "ds_read_b128 v[12:15], v244 offset:3120\n"
      "ds_read_b128 v[16:19], v244 offset:4160\n"
      "ds_read_b128 v[20:23], v244 offset:5200\n"
      "ds_read_b128 v[24:27], v244 offset:6240\n"
      "ds_read_b128 v[28:31], v244 offset:7280\n"
      "s_waitcnt lgkmcnt(7)\n"
      "v_max3_u32 v200, v0, v1, v2\n"
      "v_mov_b32 v201, v3\n"
      "s_waitcnt lgkmcnt(6)\n"
      "v_max3_u32 v200, v200, v4, v5\n"
      "v_max3_u32 v201, v201, v6, v7\n"
      "s_waitcnt lgkmcnt(5)\n"
      "v_max3_u32 v200, v200, v8, v9\n"
      "v_max3_u32 v201, v201, v10, v11\n"
      "s_waitcnt lgkmcnt(4)\n"
      "v_max3_u32 v200, v200, v12, v13\n"
      "v_max3_u32 v201, v201, v14, v15\n"
      "s_waitcnt lgkmcnt(3)\n"
      "v_max3_u32 v200, v200, v16, v17\n"
      "v_max3_u32 v201, v201, v18, v19\n"
      "s_waitcnt lgkmcnt(2)\n"
      "v_max3_u32 v200, v200, v20, v21\n"
      "v_max3_u32 v201, v201, v22, v23\n"
      "s_waitcnt lgkmcnt(1)\n"
      "v_max3_u32 v200, v200, v24, v25\n"
      "v_max3_u32 v201, v201, v26, v27\n"
      "s_waitcnt lgkmcnt(0)\n"
      "v_max3_u32 v200, v200, v28, v29\n"
      "v_max3_u32 v201, v201, v30, v31\n"
      "v_max_u32 v200, v200, v201\n"
      "v_cmp_eq_u32 vcc, -1, v200\n"
      "s_cbranch_vccnz .Lq_Rh0\n"
      ".Lq_Ch0:\n"
      "s_barrier\n"
      "s_mov_b32 s76, 0xbfb8aa3b\n"
      "ds_read_b128 v[168:171], v242 offset:0\n"
      "ds_read_b128 v[172:175], v243 offset:0\n"
      "ds_read_b128 v[176:179], v242 offset:16\n"
      "ds_read_b128 v[180:183], v243 offset:16\n"
      "s_waitcnt lgkmcnt(3)\n"
      "v_mfma_f32_16x16x32_bf16 v[112:115], a[0:3], v[168:171], 0\n"
      "ds_read_b128 v[184:187], v242 offset:32\n"
      "s_waitcnt lgkmcnt(3)\n"
      "v_mfma_f32_16x16x32_bf16 v[116:119], a[0:3], v[172:175], 0\n"
      "s_add_u32 m0, s61, 66560\n"
      "s_nop 0\n"
      "global_load_lds_dwordx4 v240, s[20:21] offset:0 sc1\n"
      "v_mfma_f32_16x16x32_bf16 v[120:123], a[4:7], v[168:171], 0\n"
      "ds_read_b128 v[188:191], v243 offset:32\n"
      "v_mfma_f32_16x16x32_bf16 v[124:127], a[4:7], v[172:175], 0\n"
      "v_mfma_f32_16x16x32_bf16 v[128:131], a[8:11], v[168:171], 0\n"
      "v_mfma_f32_16x16x32_bf16 v[132:135], a[8:11], v[172:175], 0\n"
      "s_add_u32 m0, s61, 66576\n"
      "s_nop 0\n"
      "global_load_lds_dwordx4 v240, s[20:21] offset:1024 sc1\n"
      "v_mfma_f32_16x16x32_bf16 v[136:139], a[12:15], v[168:171], 0\n"
      "s_nop 6\n"
      "s_waitcnt lgkmcnt(3)\n"
      "v_mfma_f32_16x16x32_bf16 v[112:115], a[16:19], v[176:179], v[112:115]\n"
      "ds_read_b128 v[168:171], v242 offset:48\n"
      "s_waitcnt lgkmcnt(3)\n"
      "v_mfma_f32_16x16x32_bf16 v[116:119], a[16:19], v[180:183], v[116:119]\n"
      "v_mfma_f32_16x16x32_bf16 v[120:123], a[20:23], v[176:179], v[120:123]\n"
      "ds_read_b128 v[172:175], v243 offset:48\n"
      "v_mfma_f32_16x16x32_bf16 v[124:127], a[20:23], v[180:183], v[124:127]\n"
      "s_add_u32 m0, s61, 66592\n"
      "s_nop 0\n"
      "global_load_lds_dwordx4 v240, s[20:21] offset:2048 sc1\n"
      "v_mfma_f32_16x16x32_bf16 v[128:131], a[24:27], v[176:179], v[128:131]\n"
      "v_mfma_f32_16x16x32_bf16 v[132:135], a[24:27], v[180:183], v[132:135]\n"
      "v_mfma_f32_16x16x32_bf16 v[136:139], a[28:31], v[176:179], v[136:139]\n"
      "s_nop 6\n"
      "s_waitcnt lgkmcnt(3)\n"
      "v_mfma_f32_16x16x32_bf16 v[112:115], a[32:35], v[184:187], v[112:115]\n"
      "ds_read_b128 v[176:179], v242 offset:64\n"
      "s_waitcnt lgkmcnt(3)\n"
      "v_mfma_f32_16x16x32_bf16 v[116:119], a[32:35], v[188:191], v[116:119]\n"
      "s_add_u32 m0, s61, 66608\n"
      "s_nop 0\n"
      "global_load_lds_dwordx4 v240, s[20:21] offset:3072 sc1\n"
      "v_mfma_f32_16x16x32_bf16 v[120:123], a[36:39], v[184:187], v[120:123]\n"
      "ds_read_b128 v[180:183], v243 offset:64\n"
      "v_mfma_f32_16x16x32_bf16 v[124:127], a[36:39], v[188:191], v[124:127]\n"
      "v_mfma_f32_16x16x32_bf16 v[128:131], a[40:43], v[184:187], v[128:131]\n"
      "v_mfma_f32_16x16x32_bf16 v[132:135], a[40:43], v[188:191], v[132:135]\n"
      "s_add_u32 m0, s61, 70720\n"
      "s_nop 0\n"
      "global_load_lds_dwordx4 v241, s[20:21] offset:0 sc1\n"
      "v_mfma_f32_16x16x32_bf16 v[136:139], a[44:47], v[184:187], v[136:139]\n"
      "s_nop 6\n"
      "s_waitcnt lgkmcnt(3)\n"
      "v_mfma_f32_16x16x32_bf16 v[112:115], a[48:51], v[168:171], v[112:115]\n"
      "ds_read_b128 v[184:187], v242 offset:80\n"
      "s_waitcnt lgkmcnt(3)\n"
      "v_mfma_f32_16x16x32_bf16 v[116:119], a[48:51], v[172:175], v[116:119]\n"
      "v_mfma_f32_16x16x32_bf16 v[120:123], a[52:55], v[168:171], v[120:123]\n"
      "ds_read_b128 v[188:191], v243 offset:80\n"
      "v_mfma_f32_16x16x32_bf16 v[124:127], a[52:55], v[172:175], v[124:127]\n"
      "s_add_u32 m0, s61, 70736\n"
      "s_nop 0\n"
      "global_load_lds_dwordx4 v241, s[20:21] offset:1024 sc1\n"
      "v_mfma_f32_16x16x32_bf16 v[128:131], a[56:59], v[168:171], v[128:131]\n"
      "v_mfma_f32_16x16x32_bf16 v[132:135], a[56:59], v[172:175], v[132:135]\n"
      "v_mfma_f32_16x16x32_bf16 v[136:139], a[60:63], v[168:171], v[136:139]\n"
      "s_nop 6\n"
      "s_waitcnt lgkmcnt(3)\n"
      "v_mfma_f32_16x16x32_bf16 v[112:115], a[64:67], v[176:179], v[112:115]\n"
      "ds_read_b128 v[168:171], v242 offset:96\n"
      "s_waitcnt lgkmcnt(3)\n"
      "v_mfma_f32_16x16x32_bf16 v[116:119], a[64:67], v[180:183], v[116:119]\n"
      "s_add_u32 m0, s61, 70752\n"
      "s_nop 0\n"
      "global_load_lds_dwordx4 v241, s[20:21] offset:2048 sc1\n"
      "v_mfma_f32_16x16x32_bf16 v[120:123], a[68:71], v[176:179], v[120:123]\n"
      "ds_read_b128 v[172:175], v243 offset:96\n"
      "v_mfma_f32_16x16x32_bf16 v[124:127], a[68:71], v[180:183], v[124:127]\n"
      "v_mfma_f32_16x16x32_bf16 v[128:131], a[72:75], v[176:179], v[128:131]\n"
      "v_mfma_f32_16x16x32_bf16 v[132:135], a[72:75], v[180:183], v[132:135]\n"
      "s_add_u32 m0, s61, 70768\n"
      "s_nop 0\n"
      "global_load_lds_dwordx4 v241, s[20:21] offset:3072 sc1\n"
      "v_mfma_f32_16x16x32_bf16 v[136:139], a[76:79], v[176:179], v[136:139]\n"
      "s_nop 6\n"
      "s_waitcnt lgkmcnt(3)\n"
      "v_mfma_f32_16x16x32_bf16 v[112:115], a[80:83], v[184:187], v[112:115]\n"
      "ds_read_b128 v[176:179], v242 offset:112\n"
      "s_waitcnt lgkmcnt(3)\n"
      "v_mfma_f32_16x16x32_bf16 v[116:119], a[80:83], v[188:191], v[116:119]\n"
      "v_mfma_f32_16x16x32_bf16 v[120:123], a[84:87], v[184:187], v[120:123]\n"
      "ds_read_b128 v[180:183], v243 offset:112\n"
      "v_mfma_f32_16x16x32_bf16 v[124:127], a[84:87], v[188:191], v[124:127]\n"
      "s_add_u32 m0, s61, 99840\n"
      "s_nop 0\n"
      "global_load_lds_dwordx4 v240, s[22:23] offset:0 sc1\n"
      "v_mfma_f32_16x16x32_bf16 v[128:131], a[88:91], v[184:187], v[128:131]\n"
      "v_mfma_f32_16x16x32_bf16 v[132:135], a[88:91], v[188:191], v[132:135]\n"
      "s_waitcnt vmcnt(9)\n"
      "ds_read_b128 v[80:83], v244 offset:33280\n"
      "ds_read_b128 v[84:87], v244 offset:34320\n"
      "v_mfma_f32_16x16x32_bf16 v[136:139], a[92:95], v[184:187], v[136:139]\n"
      "ds_read_b128 v[88:91], v244 offset:35360\n"
      "ds_read_b128 v[92:95], v244 offset:36400\n"
      "s_nop 4\n"
      "s_waitcnt lgkmcnt(7)\n"
      "v_mfma_f32_16x16x32_bf16 v[112:115], a[96:99], v[168:171], v[112:115]\n"
      "ds_read_b128 v[184:187], v242 offset:128\n"
      "s_waitcnt lgkmcnt(7)\n"
      "v_mfma_f32_16x16x32_bf16 v[116:119], a[96:99], v[172:175], v[116:119]\n"
      "s_add_u32 m0, s61, 99856\n"
      "s_nop 0\n"
      "global_load_lds_dwordx4 v240, s[22:23] offset:1024 sc1\n"
      "v_mfma_f32_16x16x32_bf16 v[120:123], a[100:103], v[168:171], v[120:123]\n"
      "ds_read_b128 v[188:191], v243 offset:128\n"
      "v_mfma_f32_16x16x32_bf16 v[124:127], a[100:103], v[172:175], v[124:127]\n"
      "v_mfma_f32_16x16x32_bf16 v[128:131], a[104:107], v[168:171], v[128:131]\n"
      "s_waitcnt lgkmcnt(5)\n"
      "v_max3_u32 v200, v80, v81, v82\n"
      "v_mov_b32 v201, v83\n"
      "v_mfma_f32_16x16x32_bf16 v[132:135], a[104:107], v[172:175], v[132:135]\n"
      "s_add_u32 m0, s61, 99872\n"
      "s_waitcnt lgkmcnt(4)\n"
      "v_max3_u32 v200, v200, v84, v85\n"
      "global_load_lds_dwordx4 v240, s[22:23] offset:2048 sc1\n"
      "v_mfma_f32_16x16x32_bf16 v[136:139], a[108:111], v[168:171], v[136:139]\n"
      "v_max3_u32 v201, v201, v86, v87\n"
      "s_waitcnt lgkmcnt(3)\n"
      "v_max3_u32 v200, v200, v88, v89\n"
      "s_nop 4\n"
      "v_mfma_f32_16x16x32_bf16 v[112:115], a[112:115], v[176:179], v[112:115]\n"
      "ds_read_b128 v[168:171], v242 offset:144\n"
      "v_max3_u32 v201, v201, v90, v91\n"
      "v_mfma_f32_16x16x32_bf16 v[116:119], a[112:115], v[180:183], v[116:119]\n"
      "s_waitcnt lgkmcnt(3)\n"
      "v_max3_u32 v200, v200, v92, v93\n"
      "v_max3_u32 v201, v201, v94, v95\n"
      "v_mfma_f32_16x16x32_bf16 v[120:123], a[116:119], v[176:179], v[120:123]\n"
      "ds_read_b128 v[172:175], v243 offset:144\n"
      "ds_read_b128 v[80:83], v244 offset:37440\n"
      "v_mfma_f32_16x16x32_bf16 v[124:127], a[116:119], v[180:183], v[124:127]\n"
      "s_add_u32 m0, s61, 99888\n"
      "ds_read_b128 v[84:87], v244 offset:38480\n"
      "global_load_lds_dwordx4 v240, s[22:23] offset:3072 sc1\n"
      "v_mfma_f32_16x16x32_bf16 v[128:131], a[120:123], v[176:179], v[128:131]\n"
      "ds_read_b128 v[88:91], v244 offset:39520\n"
      "ds_read_b128 v[92:95], v244 offset:40560\n"
      "v_mfma_f32_16x16x32_bf16 v[132:135], a[120:123], v[180:183], v[132:135]\n"
      "v_mfma_f32_16x16x32_bf16 v[136:139], a[124:127], v[176:179], v[136:139]\n"
      "s_nop 6\n"
      "s_waitcnt lgkmcnt(7)\n"
      "v_mfma_f32_16x16x32_bf16 v[112:115], a[128:131], v[184:187], v[112:115]\n"
      "ds_read_b128 v[176:179], v242 offset:160\n"
      "s_waitcnt lgkmcnt(7)\n"
      "v_mfma_f32_16x16x32_bf16 v[116:119], a[128:131], v[188:191], v[116:119]\n"
      "s_add_u32 m0, s61, 104000\n"
      "s_nop 0\n"
      "global_load_lds_dwordx4 v241, s[22:23] offset:0 sc1\n"
      "v_mfma_f32_16x16x32_bf16 v[120:123], a[132:135], v[184:187], v[120:123]\n"
      "ds_read_b128 v[180:183], v243 offset:160\n"
      "s_waitcnt lgkmcnt(5)\n"
      "v_max3_u32 v200, v200, v80, v81\n"
      "v_mfma_f32_16x16x32_bf16 v[124:127], a[132:135], v[188:191], v[124:127]\n"
      "v_max3_u32 v201, v201, v82, v83\n"
      "s_waitcnt lgkmcnt(4)\n"
      "v_max3_u32 v200, v200, v84, v85\n"
      "v_mfma_f32_16x16x32_bf16 v[128:131], a[136:139], v[184:187], v[128:131]\n"
      "v_max3_u32 v201, v201, v86, v87\n"
      "s_waitcnt lgkmcnt(3)\n"
      "v_max3_u32 v200, v200, v88, v89\n"
      "v_mfma_f32_16x16x32_bf16 v[132:135], a[136:139], v[188:191], v[132:135]\n"
      "s_add_u32 m0, s61, 104016\n"
      "v_max3_u32 v201, v201, v90, v91\n"
      "global_load_lds_dwordx4 v241, s[22:23] offset:1024 sc1\n"
      "v_mfma_f32_16x16x32_bf16 v[136:139], a[140:143], v[184:187], v[136:139]\n"
      "s_waitcnt lgkmcnt(2)\n"
      "v_max3_u32 v200, v200, v92, v93\n"
      "v_max3_u32 v201, v201, v94, v95\n"
      "s_nop 4\n"
      "v_mfma_f32_16x16x32_bf16 v[112:115], a[144:147], v[168:171], v[112:115]\n"
      "ds_read_b128 v[184:187], v242 offset:176\n"
      "v_max_u32 v200, v200, v201\n"
      "v_mfma_f32_16x16x32_bf16 v[116:119], a[144:147], v[172:175], v[116:119]\n"
      "v_cmp_eq_u32 vcc, -1, v200\n"
      "v_mfma_f32_16x16x32_bf16 v[120:123], a[148:151], v[168:171], v[120:123]\n"
      "ds_read_b128 v[188:191], v243 offset:176\n"
      "v_mfma_f32_16x16x32_bf16 v[124:127], a[148:151], v[172:175], v[124:127]\n"
      "s_add_u32 m0, s61, 104032\n"
      "s_nop 0\n"
      "global_load_lds_dwordx4 v241, s[22:23] offset:2048 sc1\n"
      "v_mfma_f32_16x16x32_bf16 v[128:131], a[152:155], v[168:171], v[128:131]\n"
      "s_cbranch_vccnz .Lq_Rh1\n"
      ".Lq_Ch1:\n"
      "s_barrier\n"
      "v_mfma_f32_16x16x32_bf16 v[132:135], a[152:155], v[172:175], v[132:135]\n"
      "v_mfma_f32_16x16x32_bf16 v[136:139], a[156:159], v[168:171], v[136:139]\n"
      "s_nop 6\n"
      "s_waitcnt lgkmcnt(3)\n"
      "v_mfma_f32_16x16x32_bf16 v[112:115], a[160:163], v[176:179], v[112:115]\n"
      "ds_read_b128 v[168:171], v242 offset:192\n"
      "s_waitcnt lgkmcnt(3)\n"
      "v_mfma_f32_16x16x32_bf16 v[116:119], a[160:163], v[180:183], v[116:119]\n"
      "s_add_u32 m0, s61, 104048\n"
      "s_nop 0\n"
      "global_load_lds_dwordx4 v241, s[22:23] offset:3072 sc1\n"
      "v_mfma_f32_16x16x32_bf16 v[120:123], a[164:167], v[176:179], v[120:123]\n"
      "ds_read_b128 v[172:175], v243 offset:192\n"
      "v_mfma_f32_16x16x32_bf16 v[124:127], a[164:167], v[180:183], v[124:127]\n"
      "v_mfma_f32_16x16x32_bf16 v[128:131], a[168:171], v[176:179], v[128:131]\n"
      "v_mfma_f32_16x16x32_bf16 v[132:135], a[168:171], v[180:183], v[132:135]\n"
      "global_store_dwordx4 v213, v[152:155], s[52:53] offset:0 sc1\n"
      "v_mfma_f32_16x16x32_bf16 v[136:139], a[172:175], v[176:179], v[136:139]\n"
      "s_nop 6\n"
      "s_waitcnt lgkmcnt(3)\n"
      "v_mfma_f32_16x16x32_bf16 v[112:115], a[176:179], v[184:187], v[112:115]\n"
      "ds_read_b128 v[176:179], v242 offset:208\n"
      "s_waitcnt lgkmcnt(3)\n"
      "v_mfma_f32_16x16x32_bf16 v[116:119], a[176:179], v[188:191], v[116:119]\n"
      "v_add_u32 v244, 66560, v244\n"
      "s_waitcnt vmcnt(9)\n"
      "ds_read_b128 v[224:227], v244 offset:0\n"
      "v_mfma_f32_16x16x32_bf16 v[120:123], a[180:183], v[184:187], v[120:123]\n"
      "ds_read_b128 v[180:183], v243 offset:208\n"
      "ds_read_b128 v[228:231], v244 offset:1040\n"
      "v_mfma_f32_16x16x32_bf16 v[124:127], a[180:183], v[188:191], v[124:127]\n"
      "ds_read_b128 v[232:235], v244 offset:2080\n"
      "global_store_dwordx4 v213, v[156:159], s[52:53] offset:1024 sc1\n"
      "v_mfma_f32_16x16x32_bf16 v[128:131], a[184:187], v[184:187], v[128:131]\n"
      "ds_read_b128 v[236:239], v244 offset:3120\n"
      "v_mfma_f32_16x16x32_bf16 v[132:135], a[184:187], v[188:191], v[132:135]\n"
      "v_mfma_f32_16x16x32_bf16 v[136:139], a[188:191], v[184:187], v[136:139]\n"
      "s_nop 6\n"
      "s_waitcnt lgkmcnt(7)\n"
      "v_mfma_f32_16x16x32_bf16 v[112:115], a[192:195], v[168:171], v[112:115]\n"
      "ds_read_b128 v[184:187], v242 offset:224\n"
      "s_waitcnt lgkmcnt(7)\n"
      "v_mfma_f32_16x16x32_bf16 v[116:119], a[192:195], v[172:175], v[116:119]\n"
      "global_store_dwordx4 v245, v[108:111], s[26:27] sc1\n"
      "v_mfma_f32_16x16x32_bf16 v[120:123], a[196:199], v[168:171], v[120:123]\n"
      "ds_read_b128 v[188:191], v243 offset:224\n"
      "s_waitcnt lgkmcnt(6)\n"
      "v_max3_u32 v200, v224, v225, v226\n"
      "v_mfma_f32_16x16x32_bf16 v[124:127], a[196:199], v[172:175], v[124:127]\n"
      "v_mov_b32 v201, v227\n"
      "s_waitcnt lgkmcnt(4)\n"
      "v_max3_u32 v200, v200, v228, v229\n"
      "v_mfma_f32_16x16x32_bf16 v[128:131], a[200:203], v[168:171], v[128:131]\n"
      "v_max3_u32 v201, v201, v230, v231\n"
      "s_waitcnt lgkmcnt(3)\n"
      "v_max3_u32 v200, v200, v232, v233\n"
      "v_mfma_f32_16x16x32_bf16 v[132:135], a[200:203], v[172:175], v[132:135]\n"
      "v_max3_u32 v201, v201, v234, v235\n"
      "global_load_dwordx4 v[0:3], v247, s[52:53] offset:0 sc1\n"
      "v_mfma_f32_16x16x32_bf16 v[136:139], a[204:207], v[168:171], v[136:139]\n"
      "s_waitcnt lgkmcnt(2)\n"
      "v_max3_u32 v200, v200, v236, v237\n"
      "v_max3_u32 v201, v201, v238, v239\n"
      "s_nop 4\n"
      "v_mfma_f32_16x16x32_bf16 v[112:115], a[208:211], v[176:179], v[112:115]\n"
      "ds_read_b128 v[168:171], v242 offset:240\n"
      "ds_read_b128 v[224:227], v244 offset:4160\n"
      "v_mfma_f32_16x16x32_bf16 v[116:119], a[208:211], v[180:183], v[116:119]\n"
      "ds_read_b128 v[228:231], v244 offset:5200\n"
      "ds_read_b128 v[232:235], v244 offset:6240\n"
      "v_mfma_f32_16x16x32_bf16 v[120:123], a[212:215], v[176:179], v[120:123]\n"
      "ds_read_b128 v[172:175], v243 offset:240\n"
      "ds_read_b128 v[236:239], v244 offset:7280\n"
      "v_mfma_f32_16x16x32_bf16 v[124:127], a[212:215], v[180:183], v[124:127]\n"
      "global_load_dwordx4 v[4:7], v248, s[52:53] offset:0 sc1\n"
      "v_mfma_f32_16x16x32_bf16 v[128:131], a[216:219], v[176:179], v[128:131]\n"
      "v_mfma_f32_16x16x32_bf16 v[132:135], a[216:219], v[180:183], v[132:135]\n"
      "v_mfma_f32_16x16x32_bf16 v[136:139], a[220:223], v[176:179], v[136:139]\n"
      "s_nop 6\n"
      "s_waitcnt lgkmcnt(7)\n"
      "v_mfma_f32_16x16x32_bf16 v[112:115], a[224:227], v[184:187], v[112:115]\n"
      "ds_read_b128 v[176:179], v242 offset:33280\n"
      "s_waitcnt lgkmcnt(5)\n"
      "v_max3_u32 v200, v200, v224, v225\n"
      "v_mfma_f32_16x16x32_bf16 v[116:119], a[224:227], v[188:191], v[116:119]\n"
      "v_max3_u32 v201, v201, v226, v227\n"
      "global_load_dwordx4 v[8:11], v247, s[52:53] offset:1024 sc1\n"
      "v_mfma_f32_16x16x32_bf16 v[120:123], a[228:231], v[184:187], v[120:123]\n"
      "ds_read_b128 v[180:183], v243 offset:33280\n"
      "s_waitcnt lgkmcnt(5)\n"
      "v_max3_u32 v200, v200, v228, v229\n"
      "v_mfma_f32_16x16x32_bf16 v[124:127], a[228:231], v[188:191], v[124:127]\n"
      "v_max3_u32 v201, v201, v230, v231\n"
      "s_waitcnt lgkmcnt(4)\n"
      "v_max3_u32 v200, v200, v232, v233\n"
      "v_mfma_f32_16x16x32_bf16 v[128:131], a[232:235], v[184:187], v[128:131]\n"
      "v_max3_u32 v201, v201, v234, v235\n"
      "s_waitcnt lgkmcnt(2)\n"
      "v_max3_u32 v200, v200, v236, v237\n"
      "v_mfma_f32_16x16x32_bf16 v[132:135], a[232:235], v[188:191], v[132:135]\n"
      "v_max3_u32 v201, v201, v238, v239\n"
      "global_store_dwordx4 v246, v[108:111], s[26:27] sc1\n"
      "v_mfma_f32_16x16x32_bf16 v[136:139], a[236:239], v[184:187], v[136:139]\n"
      "v_max_u32 v200, v200, v201\n"
      "v_cmp_eq_u32 vcc, -1, v200\n"
      "s_nop 4\n"
      "v_mfma_f32_16x16x32_bf16 v[112:115], a[240:243], v[168:171], v[112:115]\n"
      "ds_read_b128 v[184:187], v242 offset:33296\n"
      "v_mfma_f32_16x16x32_bf16 v[116:119], a[240:243], v[172:175], v[116:119]\n"
      "s_cbranch_vccnz .Lq_Rh2\n"
      ".Lq_Ch2:\n"
      "v_mfma_f32_16x16x32_bf16 v[120:123], a[244:247], v[168:171], v[120:123]\n"
      "ds_read_b128 v[188:191], v243 offset:33296\n"
      "v_mfma_f32_16x16x32_bf16 v[124:127], a[244:247], v[172:175], v[124:127]\n"
      "global_load_dwordx4 v[12:15], v248, s[52:53] offset:1024 sc1\n"
      "v_mfma_f32_16x16x32_bf16 v[128:131], a[248:251], v[168:171], v[128:131]\n"
      "v_mfma_f32_16x16x32_bf16 v[132:135], a[248:251], v[172:175], v[132:135]\n"
      "v_mfma_f32_16x16x32_bf16 v[136:139], a[252:255], v[168:171], v[136:139]\n"
      "s_nop 6\n"
      "s_waitcnt lgkmcnt(3)\n"
      "v_mfma_f32_16x16x32_bf16 v[140:143], a[0:3], v[176:179], 0\n"
      "ds_read_b128 v[168:171], v242 offset:33312\n"
      "s_waitcnt lgkmcnt(3)\n"
      "v_mfma_f32_16x16x32_bf16 v[144:147], a[0:3], v[180:183], 0\n"
      "global_load_dwordx4 v[16:19], v247, s[52:53] offset:2048 sc1\n"
      "v_mfma_f32_16x16x32_bf16 v[148:151], a[4:7], v[176:179], 0\n"
      "ds_read_b128 v[172:175], v243 offset:33312\n"
      "v_mul_f32 v202, s76, v112\n"
      "v_mfma_f32_16x16x32_bf16 v[152:155], a[4:7], v[180:183], 0\n"
      "v_mul_f32 v203, s76, v113\n"
      "v_mfma_f32_16x16x32_bf16 v[156:159], a[8:11], v[176:179], 0\n"
      "s_barrier\n"
      "v_exp_f32 v202, v202\n"
      "v_mfma_f32_16x16x32_bf16 v[160:163], a[8:11], v[180:183], 0\n"
      "v_exp_f32 v203, v203\n"
      "v_mfma_f32_16x16x32_bf16 v[164:167], a[12:15], v[176:179], 0\n"
      "v_mul_f32 v204, v112, v120\n"
      "global_load_dwordx4 v[20:23], v248, s[52:53] offset:2048 sc1\n"
      "s_nop 1\n"
      "s_waitcnt lgkmcnt(3)\n"
      "v_mfma_f32_16x16x32_bf16 v[140:143], a[16:19], v[184:187], v[140:143]\n"
      "ds_read_b128 v[176:179], v242 offset:33328\n"
      "v_mul_f32 v205, v113, v121\n"
      "s_waitcnt lgkmcnt(3)\n"
      "v_mfma_f32_16x16x32_bf16 v[144:147], a[16:19], v[188:191], v[144:147]\n"
      "v_permlane16_swap_b32 v128, v132\n"
      "v_mfma_f32_16x16x32_bf16 v[148:151], a[20:23], v[184:187], v[148:151]\n"
      "ds_read_b128 v[180:183], v243 offset:33328\n"
      "v_add_f32 v202, 1.0, v202\n"
      "v_mfma_f32_16x16x32_bf16 v[152:155], a[20:23], v[188:191], v[152:155]\n"
      "v_permlane16_swap_b32 v129, v133\n"
      "v_mfma_f32_16x16x32_bf16 v[156:159], a[24:27], v[184:187], v[156:159]\n"
      "v_add_f32 v203, 1.0, v203\n"
      "global_store_dwordx4 v245, v[108:111], s[28:29] sc1\n"
      "v_mfma_f32_16x16x32_bf16 v[160:163], a[24:27], v[188:191], v[160:163]\n"
      "v_permlane16_swap_b32 v130, v134\n"
      "v_mfma_f32_16x16x32_bf16 v[164:167], a[28:31], v[184:187], v[164:167]\n"
      "v_rcp_f32 v202, v202\n"
      "s_nop 1\n"
      "s_waitcnt lgkmcnt(3)\n"
      "v_mfma_f32_16x16x32_bf16 v[140:143], a[32:35], v[168:171], v[140:143]\n"
      "ds_read_b128 v[184:187], v242 offset:33344\n"
      "s_waitcnt lgkmcnt(3)\n"
      "v_mfma_f32_16x16x32_bf16 v[144:147], a[32:35], v[172:175], v[144:147]\n"
      "v_permlane16_swap_b32 v131, v135\n"
      "v_mfma_f32_16x16x32_bf16 v[148:151], a[36:39], v[168:171], v[148:151]\n"
      "ds_read_b128 v[188:191], v243 offset:33344\n"
      "v_mfma_f32_16x16x32_bf16 v[152:155], a[36:39], v[172:175], v[152:155]\n"
      "global_load_dwordx4 v[24:27], v247, s[52:53] offset:3072 sc1\n"
      "v_mfma_f32_16x16x32_bf16 v[156:159], a[40:43], v[168:171], v[156:159]\n"
      "v_rcp_f32 v203, v203\n"
      "v_mfma_f32_16x16x32_bf16 v[160:163], a[40:43], v[172:175], v[160:163]\n"
      "v_permlane16_swap_b32 v136, v138\n"
      "v_mfma_f32_16x16x32_bf16 v[164:167], a[44:47], v[168:171], v[164:167]\n"
      "v_permlane16_swap_b32 v137, v139\n"
      "s_nop 1\n"
      "s_waitcnt lgkmcnt(3)\n"
      "v_mfma_f32_16x16x32_bf16 v[140:143], a[48:51], v[176:179], v[140:143]\n"
      "ds_read_b128 v[168:171], v242 offset:33360\n"
      "v_mul_f32 v204, v204, v202\n"
      "s_waitcnt lgkmcnt(3)\n"
      "v_mfma_f32_16x16x32_bf16 v[144:147], a[48:51], v[180:183], v[144:147]\n"
      "v_mul_f32 v205, v205, v203\n"
      "global_load_dwordx4 v[28:31], v248, s[52:53] offset:3072 sc1\n"
      "v_mfma_f32_16x16x32_bf16 v[148:151], a[52:55], v[176:179], v[148:151]\n"
      "ds_read_b128 v[172:175], v243 offset:33360\n"
      "v_mul_f32 v206, s76, v136\n"
      "v_mfma_f32_16x16x32_bf16 v[152:155], a[52:55], v[180:183], v[152:155]\n"
      "v_cvt_pk_bf16_f32 v192, v204, v205\n"
      "v_mul_f32 v202, s76, v116\n"
      "v_mfma_f32_16x16x32_bf16 v[156:159], a[56:59], v[176:179], v[156:159]\n"
      "v_mul_f32 v207, s76, v137\n"
      "v_mul_f32 v203, s76, v117\n"
      "v_mfma_f32_16x16x32_bf16 v[160:163], a[56:59], v[180:183], v[160:163]\n"
      "v_exp_f32 v206, v206\n"
      "v_mfma_f32_16x16x32_bf16 v[164:167], a[60:63], v[176:179], v[164:167]\n"
      "global_load_dwordx4 v[32:35], v247, s[80:81] offset:0 sc1\n"
      "s_nop 2\n"
      "s_waitcnt lgkmcnt(3)\n"
      "v_mfma_f32_16x16x32_bf16 v[140:143], a[64:67], v[184:187], v[140:143]\n"
      "ds_read_b128 v[176:179], v242 offset:33376\n"
      "s_waitcnt lgkmcnt(3)\n"
      "v_mfma_f32_16x16x32_bf16 v[144:147], a[64:67], v[188:191], v[144:147]\n"
      "v_exp_f32 v207, v207\n"
      "v_mfma_f32_16x16x32_bf16 v[148:151], a[68:71], v[184:187], v[148:151]\n"
      "ds_read_b128 v[180:183], v243 offset:33376\n"
      "v_mul_f32 v208, v136, v138\n"
      "v_mfma_f32_16x16x32_bf16 v[152:155], a[68:71], v[188:191], v[152:155]\n"
      "v_exp_f32 v202, v202\n"
      "v_mfma_f32_16x16x32_bf16 v[156:159], a[72:75], v[184:187], v[156:159]\n"
      "v_mul_f32 v209, v137, v139\n"
      "global_store_dwordx4 v246, v[108:111], s[28:29] sc1\n"
      "v_mfma_f32_16x16x32_bf16 v[160:163], a[72:75], v[188:191], v[160:163]\n"
      "v_exp_f32 v203, v203\n"
      "v_mfma_f32_16x16x32_bf16 v[164:167], a[76:79], v[184:187], v[164:167]\n"
      "v_mul_f32 v204, v116, v124\n"
      "v_mul_f32 v205, v117, v125\n"
      "s_nop 1\n"
      "s_waitcnt lgkmcnt(3)\n"
      "v_mfma_f32_16x16x32_bf16 v[140:143], a[80:83], v[168:171], v[140:143]\n"
      "ds_read_b128 v[184:187], v242 offset:33392\n"
      "v_add_f32 v206, 1.0, v206\n"
      "s_waitcnt lgkmcnt(3)\n"
      "v_mfma_f32_16x16x32_bf16 v[144:147], a[80:83], v[172:175], v[144:147]\n"
      "v_add_f32 v207, 1.0, v207\n"
      "v_add_f32 v202, 1.0, v202\n"
      "v_mfma_f32_16x16x32_bf16 v[148:151], a[84:87], v[168:171], v[148:151]\n"
      "ds_read_b128 v[188:191], v243 offset:33392\n"
      "v_add_f32 v203, 1.0, v203\n"
      "v_mfma_f32_16x16x32_bf16 v[152:155], a[84:87], v[172:175], v[152:155]\n"
      "global_load_dwordx4 v[36:39], v248, s[80:81] offset:0 sc1\n"
      "v_mfma_f32_16x16x32_bf16 v[156:159], a[88:91], v[168:171], v[156:159]\n"
      "v_rcp_f32 v206, v206\n"
      "v_mfma_f32_16x16x32_bf16 v[160:163], a[88:91], v[172:175], v[160:163]\n"
      "v_rcp_f32 v207, v207\n"
      "v_mfma_f32_16x16x32_bf16 v[164:167], a[92:95], v[168:171], v[164:167]\n"
      "v_rcp_f32 v202, v202\n"
      "s_nop 1\n"
      "s_waitcnt lgkmcnt(3)\n"
      "v_mfma_f32_16x16x32_bf16 v[140:143], a[96:99], v[176:179], v[140:143]\n"
      "ds_read_b128 v[168:171], v242 offset:33408\n"
      "v_mul_f32 v208, v208, v206\n"
      "s_waitcnt lgkmcnt(3)\n"
      "v_mfma_f32_16x16x32_bf16 v[144:147], a[96:99], v[180:183], v[144:147]\n"
      "v_mul_f32 v209, v209, v207\n"
      "global_load_dwordx4 v[40:43], v247, s[80:81] offset:1024 sc1\n"
      "v_mfma_f32_16x16x32_bf16 v[148:151], a[100:103], v[176:179], v[148:151]\n"
      "ds_read_b128 v[172:175], v243 offset:33408\n"
      "v_cvt_pk_bf16_f32 v198, v208, v209\n"
      "v_mfma_f32_16x16x32_bf16 v[152:155], a[100:103], v[180:183], v[152:155]\n"
      "v_rcp_f32 v203, v203\n"
      "v_mfma_f32_16x16x32_bf16 v[156:159], a[104:107], v[176:179], v[156:159]\n"
      "v_mul_f32 v204, v204, v202\n"
      "v_mul_f32 v205, v205, v203\n"
      "v_mfma_f32_16x16x32_bf16 v[160:163], a[104:107], v[180:183], v[160:163]\n"
      "v_cvt_pk_bf16_f32 v199, v208, v209\n"
      "v_mul_f32 v206, s76, v128\n"
      "v_mfma_f32_16x16x32_bf16 v[164:167], a[108:111], v[176:179], v[164:167]\n"
      "v_cvt_pk_bf16_f32 v194, v204, v205\n"
      "global_load_dwordx4 v[44:47], v248, s[80:81] offset:1024 sc1\n"
      "s_nop 1\n"
      "s_waitcnt lgkmcnt(3)\n"
      "v_mfma_f32_16x16x32_bf16 v[140:143], a[112:115], v[184:187], v[140:143]\n"
      "ds_read_b128 v[176:179], v242 offset:33424\n"
      "v_mul_f32 v202, s76, v114\n"
      "s_waitcnt lgkmcnt(3)\n"
      "v_mfma_f32_16x16x32_bf16 v[144:147], a[112:115], v[188:191], v[144:147]\n"
      "v_mul_f32 v207, s76, v129\n"
      "v_mul_f32 v203, s76, v115\n"
      "v_mfma_f32_16x16x32_bf16 v[148:151], a[116:119], v[184:187], v[148:151]\n"
      "ds_read_b128 v[180:183], v243 offset:33424\n"
      "v_mfma_f32_16x16x32_bf16 v[152:155], a[116:119], v[188:191], v[152:155]\n"
      "v_exp_f32 v206, v206\n"
      "v_mfma_f32_16x16x32_bf16 v[156:159], a[120:123], v[184:187], v[156:159]\n"
      "global_store_dwordx4 v245, v[108:111], s[30:31] sc1\n"
      "v_mfma_f32_16x16x32_bf16 v[160:163], a[120:123], v[188:191], v[160:163]\n"
      "v_exp_f32 v202, v202\n"
      "v_mfma_f32_16x16x32_bf16 v[164:167], a[124:127], v[184:187], v[164:167]\n"
      "v_exp_f32 v207, v207\n"
      "s_nop 1\n"
      "s_waitcnt lgkmcnt(3)\n"
      "v_mfma_f32_16x16x32_bf16 v[140:143], a[128:131], v[168:171], v[140:143]\n"
      "ds_read_b128 v[184:187], v242 offset:33440\n"
      "v_mul_f32 v208, v128, v132\n"
      "s_waitcnt lgkmcnt(3)\n"
      "v_mfma_f32_16x16x32_bf16 v[144:147], a[128:131], v[172:175], v[144:147]\n"
      "v_exp_f32 v203, v203\n"
      "v_mfma_f32_16x16x32_bf16 v[148:151], a[132:135], v[168:171], v[148:151]\n"
      "ds_read_b128 v[188:191], v243 offset:33440\n"
      "v_mul_f32 v204, v114, v122\n"
      "v_mfma_f32_16x16x32_bf16 v[152:155], a[132:135], v[172:175], v[152:155]\n"
      "v_mul_f32 v209, v129, v133\n"
      "global_load_dwordx4 v[48:51], v247, s[80:81] offset:2048 sc1\n"
      "v_mfma_f32_16x16x32_bf16 v[156:159], a[136:139], v[168:171], v[156:159]\n"
      "v_mul_f32 v205, v115, v123\n"
      "v_add_f32 v202, 1.0, v202\n"
      "v_mfma_f32_16x16x32_bf16 v[160:163], a[136:139], v[172:175], v[160:163]\n"
      "v_add_f32 v206, 1.0, v206\n"
      "v_add_f32 v207, 1.0, v207\n"
      "v_mfma_f32_16x16x32_bf16 v[164:167], a[140:143], v[168:171], v[164:167]\n"
      "v_rcp_f32 v206, v206\n"
      "s_nop 1\n"
      "s_waitcnt lgkmcnt(3)\n"
      "v_mfma_f32_16x16x32_bf16 v[140:143], a[144:147], v[176:179], v[140:143]\n"
      "ds_read_b128 v[168:171], v242 offset:33456\n"
      "v_add_f32 v203, 1.0, v203\n"
      "s_waitcnt lgkmcnt(3)\n"
      "v_mfma_f32_16x16x32_bf16 v[144:147], a[144:147], v[180:183], v[144:147]\n"
      "global_load_dwordx4 v[52:55], v248, s[80:81] offset:2048 sc1\n"
      "v_mfma_f32_16x16x32_bf16 v[148:151], a[148:151], v[176:179], v[148:151]\n"
      "ds_read_b128 v[172:175], v243 offset:33456\n"
      "v_mfma_f32_16x16x32_bf16 v[152:155], a[148:151], v[180:183], v[152:155]\n"
      "v_rcp_f32 v202, v202\n"
      "v_mfma_f32_16x16x32_bf16 v[156:159], a[152:155], v[176:179], v[156:159]\n"
      "v_rcp_f32 v207, v207\n"
      "v_mfma_f32_16x16x32_bf16 v[160:163], a[152:155], v[180:183], v[160:163]\n"
      "v_rcp_f32 v203, v203\n"
      "v_mfma_f32_16x16x32_bf16 v[164:167], a[156:159], v[176:179], v[164:167]\n"
      "v_mul_f32 v208, v208, v206\n"
      "global_load_dwordx4 v[56:59], v247, s[80:81] offset:3072 sc1\n"
      "s_nop 1\n"
      "s_waitcnt lgkmcnt(3)\n"
      "v_mfma_f32_16x16x32_bf16 v[140:143], a[160:163], v[184:187], v[140:143]\n"
      "ds_read_b128 v[176:179], v242 offset:33472\n"
      "v_mul_f32 v204, v204, v202\n"
      "s_waitcnt lgkmcnt(3)\n"
      "v_mfma_f32_16x16x32_bf16 v[144:147], a[160:163], v[188:191], v[144:147]\n"
      "v_mul_f32 v209, v209, v207\n"
      "v_cvt_pk_bf16_f32 v196, v208, v209\n"
      "v_mfma_f32_16x16x32_bf16 v[148:151], a[164:167], v[184:187], v[148:151]\n"
      "ds_read_b128 v[180:183], v243 offset:33472\n"
      "v_mul_f32 v205, v205, v203\n"
      "v_mfma_f32_16x16x32_bf16 v[152:155], a[164:167], v[188:191], v[152:155]\n"
      "v_permlane16_swap_b32 v198, v199\n"
      "v_mfma_f32_16x16x32_bf16 v[156:159], a[168:171], v[184:187], v[156:159]\n"
      "v_cvt_pk_bf16_f32 v193, v204, v205\n"
      "global_store_dwordx4 v246, v[108:111], s[30:31] sc1\n"
      "v_mfma_f32_16x16x32_bf16 v[160:163], a[168:171], v[188:191], v[160:163]\n"
      "v_mul_f32 v202, s76, v118\n"
      "v_mul_f32 v203, s76, v119\n"
      "v_mfma_f32_16x16x32_bf16 v[164:167], a[172:175], v[184:187], v[164:167]\n"
      "v_exp_f32 v202, v202\n"
      "s_nop 1\n"
      "s_waitcnt lgkmcnt(3)\n"
      "v_mfma_f32_16x16x32_bf16 v[140:143], a[176:179], v[168:171], v[140:143]\n"
      "ds_read_b128 v[184:187], v242 offset:33488\n"
      "v_mul_f32 v206, s76, v130\n"
      "s_waitcnt lgkmcnt(3)\n"
      "v_mfma_f32_16x16x32_bf16 v[144:147], a[176:179], v[172:175], v[144:147]\n"
      "s_waitcnt vmcnt(23)\n"
      "v_exp_f32 v203, v203\n"
      "v_mfma_f32_16x16x32_bf16 v[148:151], a[180:183], v[168:171], v[148:151]\n"
      "ds_read_b128 v[188:191], v243 offset:33488\n"
      "ds_read_b128 v[224:227], v244 offset:33280\n"
      "v_mfma_f32_16x16x32_bf16 v[152:155], a[180:183], v[172:175], v[152:155]\n"
      "ds_read_b128 v[228:231], v244 offset:34320\n"
      "global_load_dwordx4 v[60:63], v248, s[80:81] offset:3072 sc1\n"
      "v_mfma_f32_16x16x32_bf16 v[156:159], a[184:187], v[168:171], v[156:159]\n"
      "ds_read_b128 v[232:235], v244 offset:35360\n"
      "ds_read_b128 v[236:239], v244 offset:36400\n"
      "v_mfma_f32_16x16x32_bf16 v[160:163], a[184:187], v[172:175], v[160:163]\n"
      "v_mul_f32 v207, s76, v131\n"
      "v_mul_f32 v204, v118, v126\n"
      "v_mfma_f32_16x16x32_bf16 v[164:167], a[188:191], v[168:171], v[164:167]\n"
      "v_exp_f32 v206, v206\n"
      "s_nop 1\n"
      "s_waitcnt lgkmcnt(7)\n"
      "v_mfma_f32_16x16x32_bf16 v[140:143], a[192:195], v[176:179], v[140:143]\n"
      "ds_read_b128 v[168:171], v242 offset:33504\n"
      "v_mul_f32 v205, v119, v127\n"
      "s_waitcnt lgkmcnt(7)\n"
      "v_mfma_f32_16x16x32_bf16 v[144:147], a[192:195], v[180:183], v[144:147]\n"
      "v_add_f32 v202, 1.0, v202\n"
      "global_load_dwordx4 v[64:67], v247, s[82:83] offset:0 sc1\n"
      "v_mfma_f32_16x16x32_bf16 v[148:151], a[196:199], v[176:179], v[148:151]\n"
      "ds_read_b128 v[172:175], v243 offset:33504\n"
      "s_waitcnt lgkmcnt(5)\n"
      "v_max3_u32 v200, v224, v225, v226\n"
      "v_mfma_f32_16x16x32_bf16 v[152:155], a[196:199], v[180:183], v[152:155]\n"
      "v_exp_f32 v207, v207\n"
      "v_mfma_f32_16x16x32_bf16 v[156:159], a[200:203], v[176:179], v[156:159]\n"
      "v_mov_b32 v201, v227\n"
      "s_waitcnt lgkmcnt(4)\n"
      "v_max3_u32 v200, v200, v228, v229\n"
      "v_mfma_f32_16x16x32_bf16 v[160:163], a[200:203], v[180:183], v[160:163]\n"
      "v_max3_u32 v201, v201, v230, v231\n"
      "s_waitcnt lgkmcnt(3)\n"
      "v_max3_u32 v200, v200, v232, v233\n"
      "v_mfma_f32_16x16x32_bf16 v[164:167], a[204:207], v[176:179], v[164:167]\n"
      "v_max3_u32 v201, v201, v234, v235\n"
      "global_load_dwordx4 v[68:71], v248, s[82:83] offset:0 sc1\n"
      "s_nop 1\n"
      "v_mfma_f32_16x16x32_bf16 v[140:143], a[208:211], v[184:187], v[140:143]\n"
      "ds_read_b128 v[176:179], v242 offset:33520\n"
      "s_waitcnt lgkmcnt(3)\n"
      "v_max3_u32 v200, v200, v236, v237\n"
      "v_mfma_f32_16x16x32_bf16 v[144:147], a[208:211], v[188:191], v[144:147]\n"
      "v_max3_u32 v201, v201, v238, v239\n"
      "ds_read_b128 v[224:227], v244 offset:37440\n"
      "v_mfma_f32_16x16x32_bf16 v[148:151], a[212:215], v[184:187], v[148:151]\n"
      "ds_read_b128 v[180:183], v243 offset:33520\n"
      "ds_read_b128 v[228:231], v244 offset:38480\n"
      "v_mfma_f32_16x16x32_bf16 v[152:155], a[212:215], v[188:191], v[152:155]\n"
      "ds_read_b128 v[232:235], v244 offset:39520\n"
      "ds_read_b128 v[236:239], v244 offset:40560\n"
      "v_mfma_f32_16x16x32_bf16 v[156:159], a[216:219], v[184:187], v[156:159]\n"
      "v_mul_f32 v208, v130, v134\n"
      "global_load_dwordx4 v[72:75], v247, s[82:83] offset:1024 sc1\n"
      "v_mfma_f32_16x16x32_bf16 v[160:163], a[216:219], v[188:191], v[160:163]\n"
      "v_mul_f32 v209, v131, v135\n"
      "v_add_f32 v206, 1.0, v206\n"
      "v_mfma_f32_16x16x32_bf16 v[164:167], a[220:223], v[184:187], v[164:167]\n"
      "v_add_f32 v203, 1.0, v203\n"
      "v_add_f32 v207, 1.0, v207\n"
      "s_nop 1\n"
      "s_waitcnt lgkmcnt(7)\n"
      "v_mfma_f32_16x16x32_bf16 v[140:143], a[224:227], v[168:171], v[140:143]\n"
      "v_add_u32 v242, 66560, v242\n"
      "s_waitcnt lgkmcnt(6)\n"
      "v_mfma_f32_16x16x32_bf16 v[144:147], a[224:227], v[172:175], v[144:147]\n"
      "v_rcp_f32 v202, v202\n"
      "v_mfma_f32_16x16x32_bf16 v[148:151], a[228:231], v[168:171], v[148:151]\n"
      "v_add_u32 v243, 66560, v243\n"
      "s_waitcnt lgkmcnt(4)\n"
      "v_max3_u32 v200, v200, v224, v225\n"
      "v_mfma_f32_16x16x32_bf16 v[152:155], a[228:231], v[172:175], v[152:155]\n"
      "v_max3_u32 v201, v201, v226, v227\n"
      "global_load_dwordx4 v[76:79], v248, s[82:83] offset:1024 sc1\n"
      "v_mfma_f32_16x16x32_bf16 v[156:159], a[232:235], v[168:171], v[156:159]\n"
      "ds_read_b128 v[184:187], v242 offset:0\n"
      "s_waitcnt lgkmcnt(3)\n"
      "v_max3_u32 v200, v200, v228, v229\n"
      "v_mfma_f32_16x16x32_bf16 v[160:163], a[232:235], v[172:175], v[160:163]\n"
      "v_rcp_f32 v206, v206\n"
      "v_mfma_f32_16x16x32_bf16 v[164:167], a[236:239], v[168:171], v[164:167]\n"
      "ds_read_b128 v[188:191], v243 offset:0\n"
      "v_max3_u32 v201, v201, v230, v231\n"
      "s_nop 1\n"
      "v_mfma_f32_16x16x32_bf16 v[140:143], a[240:243], v[176:179], v[140:143]\n"
      "ds_read_b128 v[168:171], v242 offset:16\n"
      "s_waitcnt lgkmcnt(4)\n"
      "v_max3_u32 v200, v200, v232, v233\n"
      "v_mfma_f32_16x16x32_bf16 v[144:147], a[240:243], v[180:183], v[144:147]\n"
      "v_max3_u32 v201, v201, v234, v235\n"
      "global_load_dwordx4 v[80:83], v247, s[82:83] offset:2048 sc1\n"
      "v_mfma_f32_16x16x32_bf16 v[148:151], a[244:247], v[176:179], v[148:151]\n"
      "ds_read_b128 v[172:175], v243 offset:16\n"
      "s_waitcnt lgkmcnt(4)\n"
      "v_max3_u32 v200, v200, v236, v237\n"
      "v_mfma_f32_16x16x32_bf16 v[152:155], a[244:247], v[180:183], v[152:155]\n"
      "v_rcp_f32 v203, v203\n"
      "v_mfma_f32_16x16x32_bf16 v[156:159], a[248:251], v[176:179], v[156:159]\n"
      "v_rcp_f32 v207, v207\n"
      "v_mfma_f32_16x16x32_bf16 v[160:163], a[248:251], v[180:183], v[160:163]\n"
      "v_max3_u32 v201, v201, v238, v239\n"
      "v_max_u32 v200, v200, v201\n"
      "v_mfma_f32_16x16x32_bf16 v[164:167], a[252:255], v[176:179], v[164:167]\n"
      "v_cmp_eq_u32 vcc, -1, v200\n"
      "global_load_dwordx4 v[84:87], v248, s[82:83] offset:2048 sc1\n"
      "s_nop 1\n"
      "s_waitcnt lgkmcnt(3)\n"
      "v_mfma_f32_16x16x32_bf16 v[112:115], a[0:3], v[184:187], 0\n"
      "ds_read_b128 v[176:179], v242 offset:32\n"
      "v_mul_f32 v204, v204, v202\n"
      "s_waitcnt lgkmcnt(3)\n"
      "v_mfma_f32_16x16x32_bf16 v[116:119], a[0:3], v[188:191], 0\n"
      "s_cbranch_vccnz .Lq_Rh3\n"
      ".Lq_Ch3:\n"
      "v_mfma_f32_16x16x32_bf16 v[120:123], a[4:7], v[184:187], 0\n"
      "ds_read_b128 v[180:183], v243 offset:32\n"
      "v_mul_f32 v205, v205, v203\n"
      "v_mfma_f32_16x16x32_bf16 v[124:127], a[4:7], v[188:191], 0\n"
      "v_cvt_pk_bf16_f32 v195, v204, v205\n"
      "v_mul_f32 v208, v208, v206\n"
      "v_mfma_f32_16x16x32_bf16 v[128:131], a[8:11], v[184:187], 0\n"
      "s_barrier\n"
      "v_mul_f32 v209, v209, v207\n"
      "global_load_dwordx4 v[88:91], v247, s[82:83] offset:3072 sc1\n"
      "v_mfma_f32_16x16x32_bf16 v[132:135], a[8:11], v[188:191], 0\n"
      "v_permlane16_swap_b32 v192, v194\n"
      "v_mfma_f32_16x16x32_bf16 v[136:139], a[12:15], v[184:187], 0\n"
      "v_permlane16_swap_b32 v193, v195\n"
      "s_waitcnt lgkmcnt(3)\n"
      "v_mfma_f32_16x16x32_bf16 v[112:115], a[16:19], v[168:171], v[112:115]\n"
      "ds_read_b128 v[184:187], v242 offset:48\n"
      "global_store_dwordx4 v245, v[192:195], s[24:25] sc1\n"
      "s_waitcnt lgkmcnt(3)\n"
      "v_mfma_f32_16x16x32_bf16 v[116:119], a[16:19], v[172:175], v[116:119]\n"
      "v_mul_f32 v202, s76, v140\n"
      "v_mul_f32 v203, s76, v141\n"
      "v_mfma_f32_16x16x32_bf16 v[120:123], a[20:23], v[168:171], v[120:123]\n"
      "ds_read_b128 v[188:191], v243 offset:48\n"
      "v_cvt_pk_bf16_f32 v197, v208, v209\n"
      "v_mfma_f32_16x16x32_bf16 v[124:127], a[20:23], v[172:175], v[124:127]\n"
      "global_load_dwordx4 v[92:95], v248, s[82:83] offset:3072 sc1\n"
      "v_mfma_f32_16x16x32_bf16 v[128:131], a[24:27], v[168:171], v[128:131]\n"
      "v_exp_f32 v202, v202\n"
      "v_mfma_f32_16x16x32_bf16 v[132:135], a[24:27], v[172:175], v[132:135]\n"
      "v_permlane32_swap_b32 v196, v198\n"
      "v_mfma_f32_16x16x32_bf16 v[136:139], a[28:31], v[168:171], v[136:139]\n"
      "v_exp_f32 v203, v203\n"
      "s_waitcnt lgkmcnt(3)\n"
      "v_mfma_f32_16x16x32_bf16 v[112:115], a[32:35], v[176:179], v[112:115]\n"
      "ds_read_b128 v[168:171], v242 offset:64\n"
      "v_mul_f32 v204, v140, v148\n"
      "s_waitcnt lgkmcnt(3)\n"
      "v_mfma_f32_16x16x32_bf16 v[116:119], a[32:35], v[180:183], v[116:119]\n"
      "v_mul_f32 v205, v141, v149\n"
      "global_load_dwordx4 v[96:99], v247, s[84:85] offset:0 sc1\n"
      "v_mfma_f32_16x16x32_bf16 v[120:123], a[36:39], v[176:179], v[120:123]\n"
      "ds_read_b128 v[172:175], v243 offset:64\n"
      "v_add_f32 v202, 1.0, v202\n"
      "v_mfma_f32_16x16x32_bf16 v[124:127], a[36:39], v[180:183], v[124:127]\n"
      "v_permlane32_swap_b32 v197, v199\n"
      "v_mfma_f32_16x16x32_bf16 v[128:131], a[40:43], v[176:179], v[128:131]\n"
      "global_store_dwordx4 v246, v[196:199], s[24:25] sc1\n"
      "v_add_f32 v203, 1.0, v203\n"
      "v_mfma_f32_16x16x32_bf16 v[132:135], a[40:43], v[180:183], v[132:135]\n"
      "v_permlane16_swap_b32 v156, v160\n"
      "v_mfma_f32_16x16x32_bf16 v[136:139], a[44:47], v[176:179], v[136:139]\n"
      "global_load_dwordx4 v[100:103], v248, s[84:85] offset:0 sc1\n"
      "s_waitcnt lgkmcnt(3)\n"
      "v_mfma_f32_16x16x32_bf16 v[112:115], a[48:51], v[184:187], v[112:115]\n"
      "ds_read_b128 v[176:179], v242 offset:80\n"
      "s_waitcnt lgkmcnt(3)\n"
      "v_mfma_f32_16x16x32_bf16 v[116:119], a[48:51], v[188:191], v[116:119]\n"
      "v_permlane16_swap_b32 v157, v161\n"
      "v_mfma_f32_16x16x32_bf16 v[120:123], a[52:55], v[184:187], v[120:123]\n"
      "ds_read_b128 v[180:183], v243 offset:80\n"
      "v_mfma_f32_16x16x32_bf16 v[124:127], a[52:55], v[188:191], v[124:127]\n"
      "v_permlane16_swap_b32 v158, v162\n"
      "v_mfma_f32_16x16x32_bf16 v[128:131], a[56:59], v[184:187], v[128:131]\n"
      "global_load_dwordx4 v[104:107], v247, s[84:85] offset:1024 sc1\n"
      "v_mfma_f32_16x16x32_bf16 v[132:135], a[56:59], v[188:191], v[132:135]\n"
      "v_rcp_f32 v202, v202\n"
      "v_mfma_f32_16x16x32_bf16 v[136:139], a[60:63], v[184:187], v[136:139]\n"
      "v_permlane16_swap_b32 v159, v163\n"
      "s_waitcnt lgkmcnt(3)\n"
      "v_mfma_f32_16x16x32_bf16 v[112:115], a[64:67], v[168:171], v[112:115]\n"
      "ds_read_b128 v[184:187], v242 offset:96\n"
      "s_waitcnt lgkmcnt(3)\n"
      "v_mfma_f32_16x16x32_bf16 v[116:119], a[64:67], v[172:175], v[116:119]\n"
      "v_rcp_f32 v203, v203\n"
      "v_mfma_f32_16x16x32_bf16 v[120:123], a[68:71], v[168:171], v[120:123]\n"
      "ds_read_b128 v[188:191], v243 offset:96\n"
      "v_mul_f32 v204, v204, v202\n"
      "v_mfma_f32_16x16x32_bf16 v[124:127], a[68:71], v[172:175], v[124:127]\n"
      "v_mul_f32 v205, v205, v203\n"
      "global_load_dwordx4 v[108:111], v248, s[84:85] offset:1024 sc1\n"
      "v_mfma_f32_16x16x32_bf16 v[128:131], a[72:75], v[168:171], v[128:131]\n"
      "v_permlane16_swap_b32 v164, v166\n"
      "v_mfma_f32_16x16x32_bf16 v[132:135], a[72:75], v[172:175], v[132:135]\n"
      "v_permlane16_swap_b32 v165, v167\n"
      "v_mfma_f32_16x16x32_bf16 v[136:139], a[76:79], v[168:171], v[136:139]\n"
      "v_mul_f32 v206, s76, v164\n"
      "v_mul_f32 v207, s76, v165\n"
      "s_waitcnt lgkmcnt(3)\n"
      "v_mfma_f32_16x16x32_bf16 v[112:115], a[80:83], v[176:179], v[112:115]\n"
      "ds_read_b128 v[168:171], v242 offset:112\n"
      "v_cvt_pk_bf16_f32 v192, v204, v205\n"
      "s_waitcnt lgkmcnt(3)\n"
      "v_mfma_f32_16x16x32_bf16 v[116:119], a[80:83], v[180:183], v[116:119]\n"
      "s_add_u32 m0, s62, 0\n"
      "v_mul_f32 v202, s76, v144\n"
      "global_load_lds_dwordx4 v249, s[52:53] offset:0 sc1\n"
      "v_mfma_f32_16x16x32_bf16 v[120:123], a[84:87], v[176:179], v[120:123]\n"
      "ds_read_b128 v[172:175], v243 offset:112\n"
      "v_mul_f32 v203, s76, v145\n"
      "v_mfma_f32_16x16x32_bf16 v[124:127], a[84:87], v[180:183], v[124:127]\n"
      "v_exp_f32 v206, v206\n"
      "v_mfma_f32_16x16x32_bf16 v[128:131], a[88:91], v[176:179], v[128:131]\n"
      "v_exp_f32 v207, v207\n"
      "v_mfma_f32_16x16x32_bf16 v[132:135], a[88:91], v[180:183], v[132:135]\n"
      "v_exp_f32 v202, v202\n"
      "v_mfma_f32_16x16x32_bf16 v[136:139], a[92:95], v[176:179], v[136:139]\n"
      "s_add_u32 m0, s62, 1024\n"
      "v_mul_f32 v208, v164, v166\n"
      "global_load_lds_dwordx4 v250, s[52:53] offset:0 sc1\n"
      "s_waitcnt lgkmcnt(3)\n"
      "v_mfma_f32_16x16x32_bf16 v[112:115], a[96:99], v[184:187], v[112:115]\n"
      "ds_read_b128 v[176:179], v242 offset:128\n"
      "v_mul_f32 v209, v165, v167\n"
      "s_waitcnt lgkmcnt(3)\n"
      "v_mfma_f32_16x16x32_bf16 v[116:119], a[96:99], v[188:191], v[116:119]\n"
      "v_exp_f32 v203, v203\n"
      "v_mfma_f32_16x16x32_bf16 v[120:123], a[100:103], v[184:187], v[120:123]\n"
      "ds_read_b128 v[180:183], v243 offset:128\n"
      "v_mul_f32 v204, v144, v152\n"
      "v_mfma_f32_16x16x32_bf16 v[124:127], a[100:103], v[188:191], v[124:127]\n"
      "v_add_f32 v206, 1.0, v206\n"
      "v_add_f32 v207, 1.0, v207\n"
      "v_mfma_f32_16x16x32_bf16 v[128:131], a[104:107], v[184:187], v[128:131]\n"
      "s_add_u32 m0, s62, 1024\n"
      "v_mul_f32 v205, v145, v153\n"
      "global_load_lds_dwordx4 v249, s[52:53] offset:1024 sc1\n"
      "v_mfma_f32_16x16x32_bf16 v[132:135], a[104:107], v[188:191], v[132:135]\n"
      "v_rcp_f32 v206, v206\n"
      "v_mfma_f32_16x16x32_bf16 v[136:139], a[108:111], v[184:187], v[136:139]\n"
      "v_rcp_f32 v207, v207\n"
      "s_waitcnt lgkmcnt(3)\n"
      "v_mfma_f32_16x16x32_bf16 v[112:115], a[112:115], v[168:171], v[112:115]\n"
      "ds_read_b128 v[184:187], v242 offset:144\n"
      "v_add_f32 v202, 1.0, v202\n"
      "s_waitcnt lgkmcnt(3)\n"
      "v_mfma_f32_16x16x32_bf16 v[116:119], a[112:115], v[172:175], v[116:119]\n"
      "v_add_f32 v203, 1.0, v203\n"
      "v_mul_f32 v208, v208, v206\n"
      "v_mfma_f32_16x16x32_bf16 v[120:123], a[116:119], v[168:171], v[120:123]\n"
      "ds_read_b128 v[188:191], v243 offset:144\n"
      "v_mul_f32 v209, v209, v207\n"
      "v_mfma_f32_16x16x32_bf16 v[124:127], a[116:119], v[172:175], v[124:127]\n"
      "s_add_u32 m0, s62, 2048\n"
      "v_cvt_pk_bf16_f32 v198, v208, v209\n"
      "global_load_lds_dwordx4 v250, s[52:53] offset:1024 sc1\n"
      "v_mfma_f32_16x16x32_bf16 v[128:131], a[120:123], v[168:171], v[128:131]\n"
      "v_rcp_f32 v202, v202\n"
      "v_mfma_f32_16x16x32_bf16 v[132:135], a[120:123], v[172:175], v[132:135]\n"
      "v_rcp_f32 v203, v203\n"
      "v_mfma_f32_16x16x32_bf16 v[136:139], a[124:127], v[168:171], v[136:139]\n"
      "v_mul_f32 v204, v204, v202\n"
      "v_mul_f32 v205, v205, v203\n"
      "s_waitcnt lgkmcnt(3)\n"
      "v_mfma_f32_16x16x32_bf16 v[112:115], a[128:131], v[176:179], v[112:115]\n"
      "ds_read_b128 v[168:171], v242 offset:160\n"
      "v_cvt_pk_bf16_f32 v199, v208, v209\n"
      "s_waitcnt lgkmcnt(3)\n"
      "v_mfma_f32_16x16x32_bf16 v[116:119], a[128:131], v[180:183], v[116:119]\n"
      "s_add_u32 m0, s62, 2048\n"
      "v_cvt_pk_bf16_f32 v194, v204, v205\n"
      "global_load_lds_dwordx4 v249, s[52:53] offset:2048 sc1\n"
      "v_mfma_f32_16x16x32_bf16 v[120:123], a[132:135], v[176:179], v[120:123]\n"
      "ds_read_b128 v[172:175], v243 offset:160\n"
      "v_mul_f32 v206, s76, v156\n"
      "v_mfma_f32_16x16x32_bf16 v[124:127], a[132:135], v[180:183], v[124:127]\n"
      "v_mul_f32 v202, s76, v142\n"
      "v_mul_f32 v203, s76, v143\n"
      "v_mfma_f32_16x16x32_bf16 v[128:131], a[136:139], v[176:179], v[128:131]\n"
      "v_exp_f32 v202, v202\n"
      "v_mfma_f32_16x16x32_bf16 v[132:135], a[136:139], v[180:183], v[132:135]\n"
      "v_exp_f32 v203, v203\n"
      "v_mfma_f32_16x16x32_bf16 v[136:139], a[140:143], v[176:179], v[136:139]\n"
      "s_add_u32 m0, s62, 3072\n"
      "v_mul_f32 v207, s76, v157\n"
      "global_load_lds_dwordx4 v250, s[52:53] offset:2048 sc1\n"
      "s_waitcnt lgkmcnt(3)\n"
      "v_mfma_f32_16x16x32_bf16 v[112:115], a[144:147], v[184:187], v[112:115]\n"
      "ds_read_b128 v[176:179], v242 offset:176\n"
      "s_waitcnt vmcnt(9)\n"
      "global_store_dword v210, v214, s[52:53] offset:0 sc1\n"
      "v_mul_f32 v204, v142, v150\n"
      "s_waitcnt lgkmcnt(3)\n"
      "v_mfma_f32_16x16x32_bf16 v[116:119], a[144:147], v[188:191], v[116:119]\n"
      "v_exp_f32 v206, v206\n"
      "v_mfma_f32_16x16x32_bf16 v[120:123], a[148:151], v[184:187], v[120:123]\n"
      "ds_read_b128 v[180:183], v243 offset:176\n"
      "v_mul_f32 v205, v143, v151\n"
      "v_mfma_f32_16x16x32_bf16 v[124:127], a[148:151], v[188:191], v[124:127]\n"
      "v_exp_f32 v207, v207\n"
      "v_mfma_f32_16x16x32_bf16 v[128:131], a[152:155], v[184:187], v[128:131]\n"
      "s_add_u32 m0, s62, 3072\n"
      "v_mul_f32 v208, v156, v160\n"
      "global_load_lds_dwordx4 v249, s[52:53] offset:3072 sc1\n"
      "v_mfma_f32_16x16x32_bf16 v[132:135], a[152:155], v[188:191], v[132:135]\n"
      "v_mul_f32 v209, v157, v161\n"
      "v_add_f32 v206, 1.0, v206\n"
      "v_mfma_f32_16x16x32_bf16 v[136:139], a[156:159], v[184:187], v[136:139]\n"
      "v_add_f32 v202, 1.0, v202\n"
      "v_add_f32 v203, 1.0, v203\n"
      "s_waitcnt lgkmcnt(3)\n"
      "v_mfma_f32_16x16x32_bf16 v[112:115], a[160:163], v[168:171], v[112:115]\n"
      "ds_read_b128 v[184:187], v242 offset:192\n"
      "v_add_f32 v207, 1.0, v207\n"
      "s_waitcnt lgkmcnt(3)\n"
      "v_mfma_f32_16x16x32_bf16 v[116:119], a[160:163], v[172:175], v[116:119]\n"
      "v_rcp_f32 v206, v206\n"
      "v_mfma_f32_16x16x32_bf16 v[120:123], a[164:167], v[168:171], v[120:123]\n"
      "ds_read_b128 v[188:191], v243 offset:192\n"
      "v_mfma_f32_16x16x32_bf16 v[124:127], a[164:167], v[172:175], v[124:127]\n"
      "s_add_u32 m0, s63, -3072\n"
      "s_nop 0\n"
      "global_load_lds_dwordx4 v250, s[52:53] offset:3072 sc1\n"
      "v_mfma_f32_16x16x32_bf16 v[128:131], a[168:171], v[168:171], v[128:131]\n"
      "v_rcp_f32 v202, v202\n"
      "v_mfma_f32_16x16x32_bf16 v[132:135], a[168:171], v[172:175], v[132:135]\n"
      "v_rcp_f32 v207, v207\n"
      "v_mfma_f32_16x16x32_bf16 v[136:139], a[172:175], v[168:171], v[136:139]\n"
      "v_rcp_f32 v203, v203\n"
      "s_waitcnt lgkmcnt(3)\n"
      "v_mfma_f32_16x16x32_bf16 v[112:115], a[176:179], v[176:179], v[112:115]\n"
      "ds_read_b128 v[168:171], v242 offset:208\n"
      "v_mul_f32 v208, v208, v206\n"
      "s_waitcnt lgkmcnt(3)\n"
      "v_mfma_f32_16x16x32_bf16 v[116:119], a[176:179], v[180:183], v[116:119]\n"
      "s_add_u32 m0, s63, 1024\n"
      "v_mul_f32 v204, v204, v202\n"
      "global_load_lds_dwordx4 v249, s[80:81] offset:0 sc1\n"
      "v_mfma_f32_16x16x32_bf16 v[120:123], a[180:183], v[176:179], v[120:123]\n"
      "ds_read_b128 v[172:175], v243 offset:208\n"
      "v_mul_f32 v209, v209, v207\n"
      "v_mfma_f32_16x16x32_bf16 v[124:127], a[180:183], v[180:183], v[124:127]\n"
      "v_mul_f32 v205, v205, v203\n"
      "v_cvt_pk_bf16_f32 v193, v204, v205\n"
      "v_mfma_f32_16x16x32_bf16 v[128:131], a[184:187], v[176:179], v[128:131]\n"
      "v_cvt_pk_bf16_f32 v196, v208, v209\n"
      "v_mul_f32 v202, s76, v146\n"
      "v_mfma_f32_16x16x32_bf16 v[132:135], a[184:187], v[180:183], v[132:135]\n"
      "v_permlane16_swap_b32 v198, v199\n"
      "v_mfma_f32_16x16x32_bf16 v[136:139], a[188:191], v[176:179], v[136:139]\n"
      "s_add_u32 m0, s63, 2048\n"
      "v_mul_f32 v206, s76, v158\n"
      "global_load_lds_dwordx4 v250, s[80:81] offset:0 sc1\n"
      "s_waitcnt lgkmcnt(3)\n"
      "v_mfma_f32_16x16x32_bf16 v[112:115], a[192:195], v[184:187], v[112:115]\n"
      "ds_read_b128 v[176:179], v242 offset:224\n"
      "v_mul_f32 v203, s76, v147\n"
      "s_waitcnt lgkmcnt(3)\n"
      "v_mfma_f32_16x16x32_bf16 v[116:119], a[192:195], v[188:191], v[116:119]\n"
      "v_exp_f32 v202, v202\n"
      "v_mfma_f32_16x16x32_bf16 v[120:123], a[196:199], v[184:187], v[120:123]\n"
      "ds_read_b128 v[180:183], v243 offset:224\n"
      "v_mul_f32 v207, s76, v159\n"
      "v_mfma_f32_16x16x32_bf16 v[124:127], a[196:199], v[188:191], v[124:127]\n"
      "v_exp_f32 v206, v206\n"
      "v_mfma_f32_16x16x32_bf16 v[128:131], a[200:203], v[184:187], v[128:131]\n"
      "s_add_u32 m0, s63, 2048\n"
      "s_nop 0\n"
      "global_load_lds_dwordx4 v249, s[80:81] offset:1024 sc1\n"
      "v_mfma_f32_16x16x32_bf16 v[132:135], a[200:203], v[188:191], v[132:135]\n"
      "v_exp_f32 v203, v203\n"
      "v_mfma_f32_16x16x32_bf16 v[136:139], a[204:207], v[184:187], v[136:139]\n"
      "v_exp_f32 v207, v207\n"
      "s_waitcnt lgkmcnt(3)\n"
      "v_mfma_f32_16x16x32_bf16 v[112:115], a[208:211], v[168:171], v[112:115]\n"
      "ds_read_b128 v[184:187], v242 offset:240\n"
      "v_mul_f32 v204, v146, v154\n"
      "s_waitcnt lgkmcnt(3)\n"
      "v_mfma_f32_16x16x32_bf16 v[116:119], a[208:211], v[172:175], v[116:119]\n"
      "v_mul_f32 v208, v158, v162\n"
      "v_mul_f32 v209, v159, v163\n"
      "v_mfma_f32_16x16x32_bf16 v[120:123], a[212:215], v[168:171], v[120:123]\n"
      "ds_read_b128 v[188:191], v243 offset:240\n"
      "v_mul_f32 v205, v147, v155\n"
      "v_mfma_f32_16x16x32_bf16 v[124:127], a[212:215], v[172:175], v[124:127]\n"
      "s_add_u32 m0, s63, 3072\n"
      "v_add_f32 v202, 1.0, v202\n"
      "global_load_lds_dwordx4 v250, s[80:81] offset:1024 sc1\n"
      "v_mfma_f32_16x16x32_bf16 v[128:131], a[216:219], v[168:171], v[128:131]\n"
      "v_add_f32 v206, 1.0, v206\n"
      "v_add_f32 v207, 1.0, v207\n"
      "v_mfma_f32_16x16x32_bf16 v[132:135], a[216:219], v[172:175], v[132:135]\n"
      "v_rcp_f32 v206, v206\n"
      "v_mfma_f32_16x16x32_bf16 v[136:139], a[220:223], v[168:171], v[136:139]\n"
      "v_rcp_f32 v207, v207\n"
      "s_waitcnt lgkmcnt(3)\n"
      "v_mfma_f32_16x16x32_bf16 v[112:115], a[224:227], v[176:179], v[112:115]\n"
      "ds_read_b128 v[168:171], v242 offset:33280\n"
      "v_add_f32 v203, 1.0, v203\n"
      "s_waitcnt lgkmcnt(3)\n"
      "v_mfma_f32_16x16x32_bf16 v[116:119], a[224:227], v[180:183], v[116:119]\n"
      "s_add_u32 m0, s63, 3072\n"
      "v_mul_f32 v208, v208, v206\n"
      "global_load_lds_dwordx4 v249, s[80:81] offset:2048 sc1\n"
      "v_mfma_f32_16x16x32_bf16 v[120:123], a[228:231], v[176:179], v[120:123]\n"
      "ds_read_b128 v[172:175], v243 offset:33280\n"
      "v_mul_f32 v209, v209, v207\n"
      "v_mfma_f32_16x16x32_bf16 v[124:127], a[228:231], v[180:183], v[124:127]\n"
      "v_rcp_f32 v202, v202\n"
      "v_mfma_f32_16x16x32_bf16 v[128:131], a[232:235], v[176:179], v[128:131]\n"
      "v_rcp_f32 v203, v203\n"
      "v_mfma_f32_16x16x32_bf16 v[132:135], a[232:235], v[180:183], v[132:135]\n"
      "v_mul_f32 v204, v204, v202\n"
      "v_mul_f32 v205, v205, v203\n"
      "v_mfma_f32_16x16x32_bf16 v[136:139], a[236:239], v[176:179], v[136:139]\n"
      "s_add_u32 m0, s63, 4096\n"
      "v_cvt_pk_bf16_f32 v195, v204, v205\n"
      "global_load_lds_dwordx4 v250, s[80:81] offset:2048 sc1\n"
      "s_waitcnt lgkmcnt(3)\n"
      "v_mfma_f32_16x16x32_bf16 v[112:115], a[240:243], v[184:187], v[112:115]\n"
      "ds_read_b128 v[176:179], v242 offset:33296\n"
      "v_cvt_pk_bf16_f32 v197, v208, v209\n"
      "s_waitcnt lgkmcnt(3)\n"
      "v_mfma_f32_16x16x32_bf16 v[116:119], a[240:243], v[188:191], v[116:119]\n"
      "v_permlane16_swap_b32 v192, v194\n"
      "v_mfma_f32_16x16x32_bf16 v[120:123], a[244:247], v[184:187], v[120:123]\n"
      "ds_read_b128 v[180:183], v243 offset:33296\n"
      "v_mfma_f32_16x16x32_bf16 v[124:127], a[244:247], v[188:191], v[124:127]\n"
      "v_permlane32_swap_b32 v196, v198\n"
      "v_mfma_f32_16x16x32_bf16 v[128:131], a[248:251], v[184:187], v[128:131]\n"
      "s_add_u32 m0, s63, 4096\n"
      "s_nop 0\n"
      "global_load_lds_dwordx4 v249, s[80:81] offset:3072 sc1\n"
      "v_mfma_f32_16x16x32_bf16 v[132:135], a[248:251], v[188:191], v[132:135]\n"
      "v_permlane16_swap_b32 v193, v195\n"
      "v_mfma_f32_16x16x32_bf16 v[136:139], a[252:255], v[184:187], v[136:139]\n"
      "v_permlane32_swap_b32 v197, v199\n"
      "s_waitcnt lgkmcnt(3)\n"
      "v_mfma_f32_16x16x32_bf16 v[140:143], a[0:3], v[168:171], 0\n"
      "ds_read_b128 v[184:187], v242 offset:33312\n"
      "global_store_dwordx4 v245, v[192:195], s[26:27] sc1\n"
      "s_waitcnt lgkmcnt(3)\n"
      "v_mfma_f32_16x16x32_bf16 v[144:147], a[0:3], v[172:175], 0\n"
      "global_store_dwordx4 v246, v[196:199], s[26:27] sc1\n"
      "v_mfma_f32_16x16x32_bf16 v[148:151], a[4:7], v[168:171], 0\n"
      "ds_read_b128 v[188:191], v243 offset:33312\n"
      "v_mul_f32 v202, s76, v112\n"
      "v_mfma_f32_16x16x32_bf16 v[152:155], a[4:7], v[172:175], 0\n"
      "s_add_u32 m0, s63, 30208\n"
      "v_mul_f32 v203, s76, v113\n"
      "global_load_lds_dwordx4 v250, s[80:81] offset:3072 sc1\n"
      "v_mfma_f32_16x16x32_bf16 v[156:159], a[8:11], v[168:171], 0\n"
      "s_barrier\n"
      "v_exp_f32 v202, v202\n"
      "v_mfma_f32_16x16x32_bf16 v[160:163], a[8:11], v[172:175], 0\n"
      "v_exp_f32 v203, v203\n"
      "v_mfma_f32_16x16x32_bf16 v[164:167], a[12:15], v[168:171], 0\n"
      "v_permlane16_swap_b32 v128, v132\n"
      "s_waitcnt lgkmcnt(3)\n"
      "v_mfma_f32_16x16x32_bf16 v[140:143], a[16:19], v[176:179], v[140:143]\n"
      "ds_read_b128 v[168:171], v242 offset:33328\n"
      "v_mul_f32 v204, v112, v120\n"
      "s_waitcnt lgkmcnt(3)\n"
      "v_mfma_f32_16x16x32_bf16 v[144:147], a[16:19], v[180:183], v[144:147]\n"
      "s_add_u32 m0, s63, 34304\n"
      "v_mul_f32 v205, v113, v121\n"
      "global_load_lds_dwordx4 v249, s[82:83] offset:0 sc1\n"
      "v_mfma_f32_16x16x32_bf16 v[148:151], a[20:23], v[176:179], v[148:151]\n"
      "ds_read_b128 v[172:175], v243 offset:33328\n"
      "v_add_f32 v202, 1.0, v202\n"
      "v_mfma_f32_16x16x32_bf16 v[152:155], a[20:23], v[180:183], v[152:155]\n"
      "v_permlane16_swap_b32 v129, v133\n"
      "v_mfma_f32_16x16x32_bf16 v[156:159], a[24:27], v[176:179], v[156:159]\n"
      "v_permlane16_swap_b32 v130, v134\n"
      "v_mfma_f32_16x16x32_bf16 v[160:163], a[24:27], v[180:183], v[160:163]\n"
      "v_permlane16_swap_b32 v131, v135\n"
      "v_mfma_f32_16x16x32_bf16 v[164:167], a[28:31], v[176:179], v[164:167]\n"
      "s_add_u32 m0, s63, 35328\n"
      "v_add_f32 v203, 1.0, v203\n"
      "global_load_lds_dwordx4 v250, s[82:83] offset:0 sc1\n"
      "s_waitcnt lgkmcnt(3)\n"
      "v_mfma_f32_16x16x32_bf16 v[140:143], a[32:35], v[184:187], v[140:143]\n"
      "ds_read_b128 v[176:179], v242 offset:33344\n"
      "s_waitcnt lgkmcnt(3)\n"
      "v_mfma_f32_16x16x32_bf16 v[144:147], a[32:35], v[188:191], v[144:147]\n"
      "v_rcp_f32 v202, v202\n"
      "v_mfma_f32_16x16x32_bf16 v[148:151], a[36:39], v[184:187], v[148:151]\n"
      "ds_read_b128 v[180:183], v243 offset:33344\n"
      "v_mfma_f32_16x16x32_bf16 v[152:155], a[36:39], v[188:191], v[152:155]\n"
      "v_rcp_f32 v203, v203\n"
      "v_mfma_f32_16x16x32_bf16 v[156:159], a[40:43], v[184:187], v[156:159]\n"
      "s_add_u32 m0, s63, 35328\n"
      "v_mul_f32 v204, v204, v202\n"
      "global_load_lds_dwordx4 v249, s[82:83] offset:1024 sc1\n"
      "v_mfma_f32_16x16x32_bf16 v[160:163], a[40:43], v[188:191], v[160:163]\n"
      "v_permlane16_swap_b32 v136, v138\n"
      "v_mfma_f32_16x16x32_bf16 v[164:167], a[44:47], v[184:187], v[164:167]\n"
      "v_permlane16_swap_b32 v137, v139\n"
      "s_waitcnt lgkmcnt(3)\n"
      "v_mfma_f32_16x16x32_bf16 v[140:143], a[48:51], v[168:171], v[140:143]\n"
      "ds_read_b128 v[184:187], v242 offset:33360\n"
      "v_mul_f32 v205, v205, v203\n"
      "s_waitcnt lgkmcnt(3)\n"
      "v_mfma_f32_16x16x32_bf16 v[144:147], a[48:51], v[172:175], v[144:147]\n"
      "v_mul_f32 v206, s76, v136\n"
      "v_mul_f32 v207, s76, v137\n"
      "v_mfma_f32_16x16x32_bf16 v[148:151], a[52:55], v[168:171], v[148:151]\n"
      "ds_read_b128 v[188:191], v243 offset:33360\n"
      "v_cvt_pk_bf16_f32 v192, v204, v205\n"
      "v_mfma_f32_16x16x32_bf16 v[152:155], a[52:55], v[172:175], v[152:155]\n"
      "s_add_u32 m0, s63, 36352\n"
      "v_mul_f32 v202, s76, v116\n"
      "global_load_lds_dwordx4 v250, s[82:83] offset:1024 sc1\n"
      "v_mfma_f32_16x16x32_bf16 v[156:159], a[56:59], v[168:171], v[156:159]\n"
      "v_exp_f32 v206, v206\n"
      "v_mfma_f32_16x16x32_bf16 v[160:163], a[56:59], v[172:175], v[160:163]\n"
      "v_exp_f32 v207, v207\n"
      "v_mfma_f32_16x16x32_bf16 v[164:167], a[60:63], v[168:171], v[164:167]\n"
      "v_mul_f32 v203, s76, v117\n"
      "v_mul_f32 v208, v136, v138\n"
      "s_waitcnt lgkmcnt(3)\n"
      "v_mfma_f32_16x16x32_bf16 v[140:143], a[64:67], v[176:179], v[140:143]\n"
      "ds_read_b128 v[168:171], v242 offset:33376\n"
      "v_mul_f32 v209, v137, v139\n"
      "s_waitcnt lgkmcnt(3)\n"
      "v_mfma_f32_16x16x32_bf16 v[144:147], a[64:67], v[180:183], v[144:147]\n"
      "s_add_u32 m0, s63, 36352\n"
      "v_add_f32 v206, 1.0, v206\n"
      "global_load_lds_dwordx4 v249, s[82:83] offset:2048 sc1\n"
      "v_mfma_f32_16x16x32_bf16 v[148:151], a[68:71], v[176:179], v[148:151]\n"
      "ds_read_b128 v[172:175], v243 offset:33376\n"
      "v_add_f32 v207, 1.0, v207\n"
      "v_mfma_f32_16x16x32_bf16 v[152:155], a[68:71], v[180:183], v[152:155]\n"
      "v_exp_f32 v202, v202\n"
      "v_mfma_f32_16x16x32_bf16 v[156:159], a[72:75], v[176:179], v[156:159]\n"
      "v_exp_f32 v203, v203\n"
      "v_mfma_f32_16x16x32_bf16 v[160:163], a[72:75], v[180:183], v[160:163]\n"
      "v_rcp_f32 v206, v206\n"
      "v_mfma_f32_16x16x32_bf16 v[164:167], a[76:79], v[176:179], v[164:167]\n"
      "s_add_u32 m0, s63, 37376\n"
      "v_mul_f32 v204, v116, v124\n"
      "global_load_lds_dwordx4 v250, s[82:83] offset:2048 sc1\n"
      "s_waitcnt lgkmcnt(3)\n"
      "v_mfma_f32_16x16x32_bf16 v[140:143], a[80:83], v[184:187], v[140:143]\n"
      "ds_read_b128 v[176:179], v242 offset:33392\n"
      "v_mul_f32 v205, v117, v125\n"
      "s_waitcnt lgkmcnt(3)\n"
      "v_mfma_f32_16x16x32_bf16 v[144:147], a[80:83], v[188:191], v[144:147]\n"
      "v_rcp_f32 v207, v207\n"
      "v_mfma_f32_16x16x32_bf16 v[148:151], a[84:87], v[184:187], v[148:151]\n"
      "ds_read_b128 v[180:183], v243 offset:33392\n"
      "v_add_f32 v202, 1.0, v202\n"
      "v_mfma_f32_16x16x32_bf16 v[152:155], a[84:87], v[188:191], v[152:155]\n"
      "v_add_f32 v203, 1.0, v203\n"
      "v_mul_f32 v208, v208, v206\n"
      "v_mfma_f32_16x16x32_bf16 v[156:159], a[88:91], v[184:187], v[156:159]\n"
      "global_load_dword v232, v212, s[52:53] offset:0 sc1\n"
      "s_add_u32 m0, s63, 37376\n"
      "v_mul_f32 v209, v209, v207\n"
      "global_load_lds_dwordx4 v249, s[82:83] offset:3072 sc1\n"
      "v_mfma_f32_16x16x32_bf16 v[160:163], a[88:91], v[188:191], v[160:163]\n"
      "v_rcp_f32 v202, v202\n"
      "v_mfma_f32_16x16x32_bf16 v[164:167], a[92:95], v[184:187], v[164:167]\n"
      "v_rcp_f32 v203, v203\n"
      "s_waitcnt lgkmcnt(3)\n"
      "v_mfma_f32_16x16x32_bf16 v[140:143], a[96:99], v[168:171], v[140:143]\n"
      "ds_read_b128 v[184:187], v242 offset:33408\n"
      "v_mul_f32 v204, v204, v202\n"
      "s_waitcnt lgkmcnt(3)\n"
      "v_mfma_f32_16x16x32_bf16 v[144:147], a[96:99], v[172:175], v[144:147]\n"
      "v_cvt_pk_bf16_f32 v198, v208, v209\n"
      "v_cvt_pk_bf16_f32 v199, v208, v209\n"
      "v_mfma_f32_16x16x32_bf16 v[148:151], a[100:103], v[168:171], v[148:151]\n"
      "ds_read_b128 v[188:191], v243 offset:33408\n"
      "v_mul_f32 v205, v205, v203\n"
      "v_mfma_f32_16x16x32_bf16 v[152:155], a[100:103], v[172:175], v[152:155]\n"
      "s_add_u32 m0, s63, 63488\n"
      "v_cvt_pk_bf16_f32 v194, v204, v205\n"
      "global_load_lds_dwordx4 v250, s[82:83] offset:3072 sc1\n"
      "v_mfma_f32_16x16x32_bf16 v[156:159], a[104:107], v[168:171], v[156:159]\n"
      "v_mul_f32 v206, s76, v128\n"
      "v_mul_f32 v207, s76, v129\n"
      "v_mfma_f32_16x16x32_bf16 v[160:163], a[104:107], v[172:175], v[160:163]\n"
      "v_exp_f32 v206, v206\n"
      "v_mfma_f32_16x16x32_bf16 v[164:167], a[108:111], v[168:171], v[164:167]\n"
      "v_exp_f32 v207, v207\n"
      "s_waitcnt lgkmcnt(3)\n"
      "v_mfma_f32_16x16x32_bf16 v[140:143], a[112:115], v[176:179], v[140:143]\n"
      "ds_read_b128 v[168:171], v242 offset:33424\n"
      "v_mul_f32 v202, s76, v114\n"
      "s_waitcnt lgkmcnt(3)\n"
      "v_mfma_f32_16x16x32_bf16 v[144:147], a[112:115], v[180:183], v[144:147]\n"
      "s_add_u32 m0, s63, 67584\n"
      "v_mul_f32 v203, s76, v115\n"
      "global_load_lds_dwordx4 v249, s[84:85] offset:0 sc1\n"
      "v_mfma_f32_16x16x32_bf16 v[148:151], a[116:119], v[176:179], v[148:151]\n"
      "ds_read_b128 v[172:175], v243 offset:33424\n"
      "v_mul_f32 v208, v128, v132\n"
      "v_mfma_f32_16x16x32_bf16 v[152:155], a[116:119], v[180:183], v[152:155]\n"
      "v_exp_f32 v202, v202\n"
      "v_mfma_f32_16x16x32_bf16 v[156:159], a[120:123], v[176:179], v[156:159]\n"
      "v_exp_f32 v203, v203\n"
      "v_mfma_f32_16x16x32_bf16 v[160:163], a[120:123], v[180:183], v[160:163]\n"
      "v_mul_f32 v204, v114, v122\n"
      "v_mul_f32 v205, v115, v123\n"
      "v_mfma_f32_16x16x32_bf16 v[164:167], a[124:127], v[176:179], v[164:167]\n"
      "s_add_u32 m0, s63, 68608\n"
      "v_mul_f32 v209, v129, v133\n"
      "global_load_lds_dwordx4 v250, s[84:85] offset:0 sc1\n"
      "s_waitcnt lgkmcnt(3)\n"
      "v_mfma_f32_16x16x32_bf16 v[140:143], a[128:131], v[184:187], v[140:143]\n"
      "ds_read_b128 v[176:179], v242 offset:33440\n"
      "v_add_f32 v206, 1.0, v206\n"
      "s_waitcnt lgkmcnt(3)\n"
      "v_mfma_f32_16x16x32_bf16 v[144:147], a[128:131], v[188:191], v[144:147]\n"
      "v_add_f32 v202, 1.0, v202\n"
      "v_add_f32 v203, 1.0, v203\n"
      "v_mfma_f32_16x16x32_bf16 v[148:151], a[132:135], v[184:187], v[148:151]\n"
      "ds_read_b128 v[180:183], v243 offset:33440\n"
      "v_add_f32 v207, 1.0, v207\n"
      "v_mfma_f32_16x16x32_bf16 v[152:155], a[132:135], v[188:191], v[152:155]\n"
      "v_rcp_f32 v206, v206\n"
      "v_mfma_f32_16x16x32_bf16 v[156:159], a[136:139], v[184:187], v[156:159]\n"
      "s_add_u32 m0, s63, 68608\n"
      "s_nop 0\n"
      "global_load_lds_dwordx4 v249, s[84:85] offset:1024 sc1\n"
      "v_mfma_f32_16x16x32_bf16 v[160:163], a[136:139], v[188:191], v[160:163]\n"
      "v_rcp_f32 v202, v202\n"
      "v_mfma_f32_16x16x32_bf16 v[164:167], a[140:143], v[184:187], v[164:167]\n"
      "v_rcp_f32 v207, v207\n"
      "s_waitcnt lgkmcnt(3)\n"
      "v_mfma_f32_16x16x32_bf16 v[140:143], a[144:147], v[168:171], v[140:143]\n"
      "ds_read_b128 v[184:187], v242 offset:33456\n"
      "v_mul_f32 v208, v208, v206\n"
      "s_waitcnt lgkmcnt(3)\n"
      "v_mfma_f32_16x16x32_bf16 v[144:147], a[144:147], v[172:175], v[144:147]\n"
      "v_rcp_f32 v203, v203\n"
      "v_mfma_f32_16x16x32_bf16 v[148:151], a[148:151], v[168:171], v[148:151]\n"
      "ds_read_b128 v[188:191], v243 offset:33456\n"
      "v_mul_f32 v204, v204, v202\n"
      "v_mfma_f32_16x16x32_bf16 v[152:155], a[148:151], v[172:175], v[152:155]\n"
      "s_add_u32 m0, s63, 69632\n"
      "v_mul_f32 v209, v209, v207\n"
      "global_load_lds_dwordx4 v250, s[84:85] offset:1024 sc1\n"
      "v_mfma_f32_16x16x32_bf16 v[156:159], a[152:155], v[168:171], v[156:159]\n"
      "v_mul_f32 v205, v205, v203\n"
      "v_cvt_pk_bf16_f32 v193, v204, v205\n"
      "v_mfma_f32_16x16x32_bf16 v[160:163], a[152:155], v[172:175], v[160:163]\n"
      "v_cvt_pk_bf16_f32 v196, v208, v209\n"
      "v_mul_f32 v202, s76, v118\n"
      "v_mfma_f32_16x16x32_bf16 v[164:167], a[156:159], v[168:171], v[164:167]\n"
      "v_permlane16_swap_b32 v198, v199\n"
      "s_waitcnt lgkmcnt(3)\n"
      "v_mfma_f32_16x16x32_bf16 v[140:143], a[160:163], v[176:179], v[140:143]\n"
      "ds_read_b128 v[168:171], v242 offset:33472\n"
      "v_mul_f32 v206, s76, v130\n"
      "s_waitcnt lgkmcnt(3)\n"
      "v_mfma_f32_16x16x32_bf16 v[144:147], a[160:163], v[180:183], v[144:147]\n"
      "v_mul_f32 v203, s76, v119\n"
      "v_mul_f32 v207, s76, v131\n"
      "v_mfma_f32_16x16x32_bf16 v[148:151], a[164:167], v[176:179], v[148:151]\n"
      "ds_read_b128 v[172:175], v243 offset:33472\n"
      "v_mfma_f32_16x16x32_bf16 v[152:155], a[164:167], v[180:183], v[152:155]\n"
      "v_exp_f32 v202, v202\n"
      "v_mfma_f32_16x16x32_bf16 v[156:159], a[168:171], v[176:179], v[156:159]\n"
      "v_exp_f32 v206, v206\n"
      "v_mfma_f32_16x16x32_bf16 v[160:163], a[168:171], v[180:183], v[160:163]\n"
      "v_exp_f32 v203, v203\n"
      "v_mfma_f32_16x16x32_bf16 v[164:167], a[172:175], v[176:179], v[164:167]\n"
      "v_exp_f32 v207, v207\n"
      "s_waitcnt lgkmcnt(3)\n"
      "v_mfma_f32_16x16x32_bf16 v[140:143], a[176:179], v[184:187], v[140:143]\n"
      "ds_read_b128 v[176:179], v242 offset:33488\n"
      "v_mul_f32 v204, v118, v126\n"
      "s_waitcnt lgkmcnt(3)\n"
      "v_mfma_f32_16x16x32_bf16 v[144:147], a[176:179], v[188:191], v[144:147]\n"
      "v_mul_f32 v208, v130, v134\n"
      "v_mul_f32 v209, v131, v135\n"
      "v_mfma_f32_16x16x32_bf16 v[148:151], a[180:183], v[184:187], v[148:151]\n"
      "ds_read_b128 v[180:183], v243 offset:33488\n"
      "s_waitcnt vmcnt(6)\n"
      "v_cmp_ne_u32 vcc, s67, v232\n"
      "s_cbranch_vccnz .Lq_RF0\n"
      ".Lq_CF0:\n"
      "v_mul_f32 v205, v119, v127\n"
      "v_mfma_f32_16x16x32_bf16 v[152:155], a[180:183], v[188:191], v[152:155]\n"
      "v_add_f32 v202, 1.0, v202\n"
      "global_load_dwordx4 a[0:3], v251, s[52:53] offset:0 sc1\n"
      "v_mfma_f32_16x16x32_bf16 v[156:159], a[184:187], v[184:187], v[156:159]\n"
      "v_add_f32 v206, 1.0, v206\n"
      "v_add_f32 v207, 1.0, v207\n"
      "v_mfma_f32_16x16x32_bf16 v[160:163], a[184:187], v[188:191], v[160:163]\n"
      "v_rcp_f32 v206, v206\n"
      "v_mfma_f32_16x16x32_bf16 v[164:167], a[188:191], v[184:187], v[164:167]\n"
      "v_rcp_f32 v207, v207\n"
      "s_waitcnt lgkmcnt(3)\n"
      "v_mfma_f32_16x16x32_bf16 v[140:143], a[192:195], v[168:171], v[140:143]\n"
      "ds_read_b128 v[184:187], v242 offset:33504\n"
      "v_add_f32 v203, 1.0, v203\n"
      "s_waitcnt lgkmcnt(3)\n"
      "v_mfma_f32_16x16x32_bf16 v[144:147], a[192:195], v[172:175], v[144:147]\n"
      "v_mul_f32 v208, v208, v206\n"
      "global_load_dwordx4 a[4:7], v251, s[52:53] offset:1024 sc1\n"
      "v_mfma_f32_16x16x32_bf16 v[148:151], a[196:199], v[168:171], v[148:151]\n"
      "ds_read_b128 v[188:191], v243 offset:33504\n"
      "v_mul_f32 v209, v209, v207\n"
      "v_mfma_f32_16x16x32_bf16 v[152:155], a[196:199], v[172:175], v[152:155]\n"
      "v_rcp_f32 v202, v202\n"
      "v_mfma_f32_16x16x32_bf16 v[156:159], a[200:203], v[168:171], v[156:159]\n"
      "v_rcp_f32 v203, v203\n"
      "v_mfma_f32_16x16x32_bf16 v[160:163], a[200:203], v[172:175], v[160:163]\n"
      "s_barrier\n"
      "global_load_dwordx4 v[224:227], v218, s[52:53] offset:0 sc1\n"
      "global_load_dwordx4 v[228:231], v219, s[52:53] offset:0 sc1\n"
      "v_mul_f32 v204, v204, v202\n"
      "v_mul_f32 v205, v205, v203\n"
      "v_mfma_f32_16x16x32_bf16 v[164:167], a[204:207], v[168:171], v[164:167]\n"
      "v_cvt_pk_bf16_f32 v195, v204, v205\n"
      "global_load_dwordx4 a[8:11], v251, s[52:53] offset:2048 sc1\n"
      "s_waitcnt lgkmcnt(3)\n"
      "v_mfma_f32_16x16x32_bf16 v[140:143], a[208:211], v[176:179], v[140:143]\n"
      "ds_read_b128 v[168:171], v242 offset:33520\n"
      "v_cvt_pk_bf16_f32 v197, v208, v209\n"
      "s_waitcnt lgkmcnt(3)\n"
      "v_mfma_f32_16x16x32_bf16 v[144:147], a[208:211], v[180:183], v[144:147]\n"
      "v_permlane16_swap_b32 v192, v194\n"
      "v_mfma_f32_16x16x32_bf16 v[148:151], a[212:215], v[176:179], v[148:151]\n"
      "ds_read_b128 v[172:175], v243 offset:33520\n"
      "v_mfma_f32_16x16x32_bf16 v[152:155], a[212:215], v[180:183], v[152:155]\n"
      "v_permlane32_swap_b32 v196, v198\n"
      "v_mfma_f32_16x16x32_bf16 v[156:159], a[216:219], v[176:179], v[156:159]\n"
      "global_load_dwordx4 a[12:15], v251, s[52:53] offset:3072 sc1\n"
      "v_mfma_f32_16x16x32_bf16 v[160:163], a[216:219], v[180:183], v[160:163]\n"
      "v_permlane16_swap_b32 v193, v195\n"
      "v_mfma_f32_16x16x32_bf16 v[164:167], a[220:223], v[176:179], v[164:167]\n"
      "v_permlane32_swap_b32 v197, v199\n"
      "s_waitcnt lgkmcnt(3)\n"
      "v_mfma_f32_16x16x32_bf16 v[140:143], a[224:227], v[184:187], v[140:143]\n"
      "global_store_dwordx4 v245, v[192:195], s[28:29] sc1\n"
      "global_store_dwordx4 v246, v[196:199], s[28:29] sc1\n"
      "s_waitcnt lgkmcnt(2)\n"
      "v_mfma_f32_16x16x32_bf16 v[144:147], a[224:227], v[188:191], v[144:147]\n"
      "v_mfma_f32_16x16x32_bf16 v[148:151], a[228:231], v[184:187], v[148:151]\n"
      "global_load_dwordx4 a[16:19], v251, s[80:81] offset:0 sc1\n"
      "v_mfma_f32_16x16x32_bf16 v[152:155], a[228:231], v[188:191], v[152:155]\n"
      "v_mfma_f32_16x16x32_bf16 v[156:159], a[232:235], v[184:187], v[156:159]\n"
      "v_mfma_f32_16x16x32_bf16 v[160:163], a[232:235], v[188:191], v[160:163]\n"
      "v_mfma_f32_16x16x32_bf16 v[164:167], a[236:239], v[184:187], v[164:167]\n"
      "s_waitcnt lgkmcnt(1)\n"
      "v_mfma_f32_16x16x32_bf16 v[140:143], a[240:243], v[168:171], v[140:143]\n"
      "global_load_dwordx4 a[20:23], v251, s[80:81] offset:1024 sc1\n"
      "s_waitcnt lgkmcnt(0)\n"
      "v_mfma_f32_16x16x32_bf16 v[144:147], a[240:243], v[172:175], v[144:147]\n"
      "v_mfma_f32_16x16x32_bf16 v[148:151], a[244:247], v[168:171], v[148:151]\n"
      "v_mfma_f32_16x16x32_bf16 v[152:155], a[244:247], v[172:175], v[152:155]\n"
      "v_mfma_f32_16x16x32_bf16 v[156:159], a[248:251], v[168:171], v[156:159]\n"
      "v_mfma_f32_16x16x32_bf16 v[160:163], a[248:251], v[172:175], v[160:163]\n"
      "global_load_dwordx4 a[24:27], v251, s[80:81] offset:2048 sc1\n"
      "v_mfma_f32_16x16x32_bf16 v[164:167], a[252:255], v[168:171], v[164:167]\n"
      "v_mul_f32 v202, s76, v140\n"
      "global_load_dwordx4 a[28:31], v251, s[80:81] offset:3072 sc1\n"
      "v_mul_f32 v203, s76, v141\n"
      "s_nop 2\n"
      "v_permlane16_swap_b32 v156, v160\n"
      "v_permlane16_swap_b32 v157, v161\n"
      "v_exp_f32 v202, v202\n"
      "v_exp_f32 v203, v203\n"
      "v_permlane16_swap_b32 v158, v162\n"
      "v_permlane16_swap_b32 v159, v163\n"
      "v_mul_f32 v204, v140, v148\n"
      "v_mul_f32 v205, v141, v149\n"
      "v_permlane16_swap_b32 v164, v166\n"
      "v_permlane16_swap_b32 v165, v167\n"
      "global_load_dwordx4 a[32:35], v251, s[82:83] offset:0 sc1\n"
      "v_add_f32 v202, 1.0, v202\n"
      "v_add_f32 v203, 1.0, v203\n"
      "v_mul_f32 v206, s76, v164\n"
      "v_mul_f32 v207, s76, v165\n"
      "v_rcp_f32 v202, v202\n"
      "v_rcp_f32 v203, v203\n"
      "v_exp_f32 v206, v206\n"
      "v_exp_f32 v207, v207\n"
      "v_mul_f32 v204, v204, v202\n"
      "v_mul_f32 v205, v205, v203\n"
      "v_mul_f32 v208, v164, v166\n"
      "v_mul_f32 v209, v165, v167\n"
      "v_cvt_pk_bf16_f32 v192, v204, v205\n"
      "v_mul_f32 v202, s76, v144\n"
      "global_load_dwordx4 a[36:39], v251, s[82:83] offset:1024 sc1\n"
      "v_add_f32 v206, 1.0, v206\n"
      "v_add_f32 v207, 1.0, v207\n"
      "v_mul_f32 v203, s76, v145\n"
      "v_exp_f32 v202, v202\n"
      "v_rcp_f32 v206, v206\n"
      "v_rcp_f32 v207, v207\n"
      "v_exp_f32 v203, v203\n"
      "v_mul_f32 v204, v144, v152\n"
      "v_mul_f32 v208, v208, v206\n"
      "v_mul_f32 v209, v209, v207\n"
      "v_mul_f32 v205, v145, v153\n"
      "v_add_f32 v202, 1.0, v202\n"
      "v_cvt_pk_bf16_f32 v198, v208, v209\n"
      "v_cvt_pk_bf16_f32 v199, v208, v209\n"
      "global_load_dwordx4 a[40:43], v251, s[82:83] offset:2048 sc1\n"
      "v_add_f32 v203, 1.0, v203\n"
      "v_rcp_f32 v202, v202\n"
      "v_mul_f32 v206, s76, v156\n"
      "v_mul_f32 v207, s76, v157\n"
      "v_rcp_f32 v203, v203\n"
      "v_mul_f32 v204, v204, v202\n"
      "v_exp_f32 v206, v206\n"
      "v_exp_f32 v207, v207\n"
      "v_mul_f32 v205, v205, v203\n"
      "v_cvt_pk_bf16_f32 v194, v204, v205\n"
      "v_mul_f32 v208, v156, v160\n"
      "v_mul_f32 v209, v157, v161\n"
      "v_mul_f32 v202, s76, v142\n"
      "v_mul_f32 v203, s76, v143\n"
      "global_load_dwordx4 a[44:47], v251, s[82:83] offset:3072 sc1\n"
      "v_add_f32 v206, 1.0, v206\n"
      "v_add_f32 v207, 1.0, v207\n"
      "v_exp_f32 v202, v202\n"
      "v_exp_f32 v203, v203\n"
      "v_rcp_f32 v206, v206\n"
      "v_rcp_f32 v207, v207\n"
      "v_add_u32 v223, 66560, v255\n"
      "s_waitcnt vmcnt(15)\n"
      "v_mfma_f32_32x32x16_bf16 v[112:127], v[0:3], a[0:3], 0\n"
      "global_load_dwordx4 a[48:51], v251, s[84:85] offset:0 sc1\n"
      "v_mul_f32 v204, v142, v150\n"
      "v_mul_f32 v205, v143, v151\n"
      "v_mul_f32 v208, v208, v206\n"
      "v_mul_f32 v209, v209, v207\n"
      "v_mfma_f32_32x32x16_bf16 v[168:183], v[4:7], a[0:3], 0\n"
      "v_add_f32 v202, 1.0, v202\n"
      "v_add_f32 v203, 1.0, v203\n"
      "v_cvt_pk_bf16_f32 v196, v208, v209\n"
      "v_permlane16_swap_b32 v198, v199\n"
      "s_waitcnt vmcnt(15)\n"
      "v_mfma_f32_32x32x16_bf16 v[112:127], v[8:11], a[4:7], v[112:127]\n"
      "global_load_dwordx4 a[52:55], v251, s[84:85] offset:1024 sc1\n"
      "v_rcp_f32 v202, v202\n"
      "v_rcp_f32 v203, v203\n"
      "v_mfma_f32_32x32x16_bf16 v[168:183], v[12:15], a[4:7], v[168:183]\n"
      "v_mul_f32 v206, s76, v158\n"
      "v_mul_f32 v207, s76, v159\n"
      "v_mul_f32 v204, v204, v202\n"
      "v_mul_f32 v205, v205, v203\n"
      "s_waitcnt vmcnt(13)\n"
      "v_mfma_f32_32x32x16_bf16 v[112:127], v[16:19], a[8:11], v[112:127]\n"
      "global_load_dwordx4 a[56:59], v252, s[52:53] offset:0 sc1\n"
      "v_exp_f32 v206, v206\n"
      "v_exp_f32 v207, v207\n"
      "v_mfma_f32_32x32x16_bf16 v[168:183], v[20:23], a[8:11], v[168:183]\n"
      "v_cvt_pk_bf16_f32 v193, v204, v205\n"
      "v_mul_f32 v202, s76, v146\n"
      "v_mul_f32 v208, v158, v162\n"
      "v_mul_f32 v209, v159, v163\n"
      "v_mul_f32 v203, s76, v147\n"
      "s_waitcnt vmcnt(13)\n"
      "v_mfma_f32_32x32x16_bf16 v[112:127], v[24:27], a[12:15], v[112:127]\n"
      "global_load_dwordx4 a[60:63], v252, s[52:53] offset:1024 sc1\n"
      "v_exp_f32 v202, v202\n"
      "v_add_f32 v206, 1.0, v206\n"
      "v_add_f32 v207, 1.0, v207\n"
      "v_mfma_f32_32x32x16_bf16 v[168:183], v[28:31], a[12:15], v[168:183]\n"
      "v_exp_f32 v203, v203\n"
      "v_mul_f32 v204, v146, v154\n"
      "v_rcp_f32 v206, v206\n"
      "s_waitcnt vmcnt(11)\n"
      "v_mfma_f32_32x32x16_bf16 v[112:127], v[32:35], a[16:19], v[112:127]\n"
      "global_load_dwordx4 a[64:67], v252, s[52:53] offset:2048 sc1\n"
      "v_rcp_f32 v207, v207\n"
      "v_mul_f32 v205, v147, v155\n"
      "v_add_f32 v202, 1.0, v202\n"
      "v_mfma_f32_32x32x16_bf16 v[168:183], v[36:39], a[16:19], v[168:183]\n"
      "v_mul_f32 v208, v208, v206\n"
      "v_mul_f32 v209, v209, v207\n"
      "v_add_f32 v203, 1.0, v203\n"
      "v_rcp_f32 v202, v202\n"
      "s_waitcnt vmcnt(11)\n"
      "v_mfma_f32_32x32x16_bf16 v[112:127], v[40:43], a[20:23], v[112:127]\n"
      "global_load_dwordx4 a[68:71], v252, s[52:53] offset:3072 sc1\n"
      "v_cvt_pk_bf16_f32 v197, v208, v209\n"
      "v_permlane32_swap_b32 v196, v198\n"
      "v_mfma_f32_32x32x16_bf16 v[168:183], v[44:47], a[20:23], v[168:183]\n"
      "v_rcp_f32 v203, v203\n"
      "v_mul_f32 v204, v204, v202\n"
      "v_permlane32_swap_b32 v197, v199\n"
      "s_waitcnt vmcnt(11)\n"
      "v_mfma_f32_32x32x16_bf16 v[112:127], v[48:51], a[24:27], v[112:127]\n"
      "global_load_dwordx4 a[72:75], v252, s[80:81] offset:0 sc1\n"
      "global_store_dwordx4 v246, v[196:199], s[30:31] sc1\n"
      "v_mul_f32 v205, v205, v203\n"
      "v_cvt_pk_bf16_f32 v195, v204, v205\n"
      "v_mfma_f32_32x32x16_bf16 v[168:183], v[52:55], a[24:27], v[168:183]\n"
      "v_permlane16_swap_b32 v192, v194\n"
      "v_permlane16_swap_b32 v193, v195\n"
      "global_store_dwordx4 v245, v[192:195], s[30:31] sc1\n"
      "s_waitcnt vmcnt(13)\n"
      "v_mfma_f32_32x32x16_bf16 v[112:127], v[56:59], a[28:31], v[112:127]\n"
      "global_load_dwordx4 a[76:79], v252, s[80:81] offset:1024 sc1\n"
      "v_mfma_f32_32x32x16_bf16 v[168:183], v[60:63], a[28:31], v[168:183]\n"
      "s_waitcnt vmcnt(13)\n"
      "v_mfma_f32_32x32x16_bf16 v[112:127], v[64:67], a[32:35], v[112:127]\n"
      "global_load_dwordx4 a[80:83], v252, s[80:81] offset:2048 sc1\n"
      "v_mfma_f32_32x32x16_bf16 v[168:183], v[68:71], a[32:35], v[168:183]\n"
      "s_waitcnt vmcnt(13)\n"
      "v_mfma_f32_32x32x16_bf16 v[112:127], v[72:75], a[36:39], v[112:127]\n"
      "global_load_dwordx4 a[84:87], v252, s[80:81] offset:3072 sc1\n"
      "v_mfma_f32_32x32x16_bf16 v[168:183], v[76:79], a[36:39], v[168:183]\n"
      "s_waitcnt vmcnt(13)\n"
      "v_mfma_f32_32x32x16_bf16 v[112:127], v[80:83], a[40:43], v[112:127]\n"
      "s_branch .Lq_Lsk_g5\n"
      ".Lq_Rg14:\n"
      "s_sub_u32 s69, s69, 1\n"
      "s_cbranch_scc1 .Lq_Tg14\n"
      "s_call_b64 s[2:3], .Lq_Lch14\n"
      "v_accvgpr_read_b32 v184, a56\n"
      "v_accvgpr_read_b32 v185, a60\n"
      "v_max_u32 v184, v184, v185\n"
      "v_cmp_eq_u32 vcc, -1, v184\n"
      "s_cbranch_vccnz .Lq_Rg14\n"
      "s_branch .Lq_Cg14\n"
      ".Lq_Tg14:\n"
      "s_mov_b32 s69, 0\n"
      "s_branch .Lq_Cg14\n"
      ".Lq_Rg16:\n"
      "s_sub_u32 s69, s69, 1\n"
      "s_cbranch_scc1 .Lq_Tg16\n"
      "s_call_b64 s[2:3], .Lq_Lch16\n"
      "v_accvgpr_read_b32 v184, a64\n"
      "v_accvgpr_read_b32 v185, a68\n"
      "v_max_u32 v184, v184, v185\n"
      "v_cmp_eq_u32 vcc, -1, v184\n"
      "s_cbranch_vccnz .Lq_Rg16\n"
      "s_branch .Lq_Cg16\n"
      ".Lq_Tg16:\n"
      "s_mov_b32 s69, 0\n"
      "s_branch .Lq_Cg16\n"
      ".Lq_Rg18:\n"
      "s_sub_u32 s69, s69, 1\n"
      "s_cbranch_scc1 .Lq_Tg18\n"
      "s_call_b64 s[2:3], .Lq_Lch18\n"
      "v_accvgpr_read_b32 v184, a72\n"
      "v_accvgpr_read_b32 v185, a76\n"
      "v_max_u32 v184, v184, v185\n"
      "v_cmp_eq_u32 vcc, -1, v184\n"
      "s_cbranch_vccnz .Lq_Rg18\n"
      "s_branch .Lq_Cg18\n"
      ".Lq_Tg18:\n"
      "s_mov_b32 s69, 0\n"
      "s_branch .Lq_Cg18\n"
      ".Lq_Rg20:\n"
      "s_sub_u32 s69, s69, 1\n"
      "s_cbranch_scc1 .Lq_Tg20\n"
      "s_call_b64 s[2:3], .Lq_Lch20\n"
      "v_accvgpr_read_b32 v184, a80\n"
      "v_accvgpr_read_b32 v185, a84\n"
      "v_max_u32 v184, v184, v185\n"
      "v_cmp_eq_u32 vcc, -1, v184\n"
      "s_cbranch_vccnz .Lq_Rg20\n"
      "s_branch .Lq_Cg20\n"
      ".Lq_Tg20:\n"
      "s_mov_b32 s69, 0\n"
      "s_branch .Lq_Cg20\n"
      ".Lq_Rg22:\n"
      "s_sub_u32 s69, s69, 1\n"
      "s_cbranch_scc1 .Lq_Tg22\n"
      "s_call_b64 s[2:3], .Lq_Lch22\n"
      "v_accvgpr_read_b32 v184, a88\n"
      "v_accvgpr_read_b32 v185, a92\n"
      "v_max_u32 v184, v184, v185\n"
      "v_cmp_eq_u32 vcc, -1, v184\n"
      "s_cbranch_vccnz .Lq_Rg22\n"
      "s_branch .Lq_Cg22\n"
      ".Lq_Tg22:\n"
      "s_mov_b32 s69, 0\n"
      "s_branch .Lq_Cg22\n"
      ".Lq_Lsk_g5:\n"
      "global_load_dwordx4 a[88:91], v252, s[82:83] offset:0 sc1\n"
      "v_mfma_f32_32x32x16_bf16 v[168:183], v[84:87], a[40:43], v[168:183]\n"
      "s_waitcnt vmcnt(13)\n"
      "v_mfma_f32_32x32x16_bf16 v[112:127], v[88:91], a[44:47], v[112:127]\n"
      "global_load_dwordx4 a[92:95], v252, s[82:83] offset:1024 sc1\n"
      "v_mfma_f32_32x32x16_bf16 v[168:183], v[92:95], a[44:47], v[168:183]\n"
      "s_waitcnt vmcnt(13)\n"
      "v_mfma_f32_32x32x16_bf16 v[112:127], v[96:99], a[48:51], v[112:127]\n"
      "global_load_dwordx4 a[96:99], v252, s[82:83] offset:2048 sc1\n"
      "v_mfma_f32_32x32x16_bf16 v[168:183], v[100:103], a[48:51], v[168:183]\n"
      "s_waitcnt vmcnt(13)\n"
      "v_mfma_f32_32x32x16_bf16 v[112:127], v[104:107], a[52:55], v[112:127]\n"
      "global_load_dwordx4 a[100:103], v252, s[82:83] offset:3072 sc1\n"
      "s_waitcnt vmcnt(13)\n"
      "v_accvgpr_read_b32 v184, a56\n"
      "s_waitcnt vmcnt(12)\n"
      "v_accvgpr_read_b32 v185, a60\n"
      "v_max_u32 v184, v184, v185\n"
      "v_cmp_eq_u32 vcc, -1, v184\n"
      "v_mfma_f32_32x32x16_bf16 v[168:183], v[108:111], a[52:55], v[168:183]\n"
      "s_cbranch_vccnz .Lq_Rg14\n"
      ".Lq_Cg14:\n"
      "v_mfma_f32_32x32x16_bf16 v[128:143], v[0:3], a[56:59], 0\n"
      "global_load_dwordx4 a[104:107], v252, s[84:85] offset:0 sc1\n"
      "v_mfma_f32_32x32x16_bf16 v[144:159], v[4:7], a[56:59], 0\n"
      "v_mfma_f32_32x32x16_bf16 v[128:143], v[8:11], a[60:63], v[128:143]\n"
      "global_load_dwordx4 a[108:111], v252, s[84:85] offset:1024 sc1\n"
      "s_waitcnt vmcnt(13)\n"
      "v_accvgpr_read_b32 v184, a64\n"
      "s_waitcnt vmcnt(12)\n"
      "v_accvgpr_read_b32 v185, a68\n"
      "v_max_u32 v184, v184, v185\n"
      "v_cmp_eq_u32 vcc, -1, v184\n"
      "v_mfma_f32_32x32x16_bf16 v[144:159], v[12:15], a[60:63], v[144:159]\n"
      "s_cbranch_vccnz .Lq_Rg16\n"
      ".Lq_Cg16:\n"
      "v_mfma_f32_32x32x16_bf16 v[128:143], v[16:19], a[64:67], v[128:143]\n"
      "global_load_dwordx4 a[112:115], v253, s[52:53] offset:0 sc1\n"
      "ds_read_b128 v[0:3], v222 offset:0\n"
      "ds_read_b128 v[4:7], v222 offset:1024\n"
      "ds_read_b128 v[8:11], v222 offset:2048\n"
      "ds_read_b128 v[12:15], v222 offset:3072\n"
      "v_mfma_f32_32x32x16_bf16 v[144:159], v[20:23], a[64:67], v[144:159]\n"
      "v_mfma_f32_32x32x16_bf16 v[128:143], v[24:27], a[68:71], v[128:143]\n"
      "global_load_dwordx4 a[116:119], v253, s[52:53] offset:1024 sc1\n"
      "s_waitcnt vmcnt(13)\n"
      "v_accvgpr_read_b32 v184, a72\n"
      "s_waitcnt vmcnt(10)\n"
      "v_accvgpr_read_b32 v185, a76\n"
      "v_max_u32 v184, v184, v185\n"
      "v_cmp_eq_u32 vcc, -1, v184\n"
      "v_mfma_f32_32x32x16_bf16 v[144:159], v[28:31], a[68:71], v[144:159]\n"
      "s_cbranch_vccnz .Lq_Rg18\n"
      ".Lq_Cg18:\n"
      "v_mfma_f32_32x32x16_bf16 v[128:143], v[32:35], a[72:75], v[128:143]\n"
      "global_load_dwordx4 a[120:123], v253, s[52:53] offset:2048 sc1\n"
      "ds_read_b128 v[16:19], v222 offset:4096\n"
      "ds_read_b128 v[20:23], v222 offset:5120\n"
      "ds_read_b128 v[24:27], v222 offset:6144\n"
      "ds_read_b128 v[28:31], v255 offset:0\n"
      "v_mfma_f32_32x32x16_bf16 v[144:159], v[36:39], a[72:75], v[144:159]\n"
      "v_mfma_f32_32x32x16_bf16 v[128:143], v[40:43], a[76:79], v[128:143]\n"
      "global_load_dwordx4 a[124:127], v253, s[52:53] offset:3072 sc1\n"
      "s_waitcnt vmcnt(11)\n"
      "v_accvgpr_read_b32 v184, a80\n"
      "s_waitcnt vmcnt(10)\n"
      "v_accvgpr_read_b32 v185, a84\n"
      "v_max_u32 v184, v184, v185\n"
      "v_cmp_eq_u32 vcc, -1, v184\n"
      "v_mfma_f32_32x32x16_bf16 v[144:159], v[44:47], a[76:79], v[144:159]\n"
      "s_cbranch_vccnz .Lq_Rg20\n"
      ".Lq_Cg20:\n"
      "v_mfma_f32_32x32x16_bf16 v[128:143], v[48:51], a[80:83], v[128:143]\n"
      "s_branch .Lq_Lsk_g10\n"
      ".Lq_Rg24:\n"
      "s_sub_u32 s69, s69, 1\n"
      "s_cbranch_scc1 .Lq_Tg24\n"
      "s_call_b64 s[2:3], .Lq_Lch24\n"
      "v_accvgpr_read_b32 v184, a96\n"
      "v_accvgpr_read_b32 v185, a100\n"
      "v_max_u32 v184, v184, v185\n"
      "v_cmp_eq_u32 vcc, -1, v184\n"
      "s_cbranch_vccnz .Lq_Rg24\n"
      "s_branch .Lq_Cg24\n"
      ".Lq_Tg24:\n"
      "s_mov_b32 s69, 0\n"
      "s_branch .Lq_Cg24\n"
      ".Lq_Rg26:\n"
      "s_sub_u32 s69, s69, 1\n"
      "s_cbranch_scc1 .Lq_Tg26\n"
      "s_call_b64 s[2:3], .Lq_Lch26\n"
      "v_accvgpr_read_b32 v184, a104\n"
      "v_accvgpr_read_b32 v185, a108\n"
      "v_max_u32 v184, v184, v185\n"
      "v_cmp_eq_u32 vcc, -1, v184\n"
      "s_cbranch_vccnz .Lq_Rg26\n"
      "s_branch .Lq_Cg26\n"
      ".Lq_Tg26:\n"
      "s_mov_b32 s69, 0\n"
      "s_branch .Lq_Cg26\n"
      ".Lq_Rg28:\n"
      "s_sub_u32 s69, s69, 1\n"
      "s_cbranch_scc1 .Lq_Tg28\n"
      "s_call_b64 s[2:3], .Lq_Lch28\n"
      "v_accvgpr_read_b32 v184, a112\n"
      "v_accvgpr_read_b32 v185, a116\n"
      "v_max_u32 v184, v184, v185\n"
      "v_cmp_eq_u32 vcc, -1, v184\n"
      "s_cbranch_vccnz .Lq_Rg28\n"
      "s_branch .Lq_Cg28\n"
      ".Lq_Tg28:\n"
      "s_mov_b32 s69, 0\n"
      "s_branch .Lq_Cg28\n"
      ".Lq_Rg30:\n"
      "s_sub_u32 s69, s69, 1\n"
      "s_cbranch_scc1 .Lq_Tg30\n"
      "s_call_b64 s[2:3], .Lq_Lch30\n"
      "v_accvgpr_read_b32 v184, a120\n"
      "v_accvgpr_read_b32 v185, a124\n"
      "v_max_u32 v184, v184, v185\n"
      "v_cmp_eq_u32 vcc, -1, v184\n"
      "s_cbranch_vccnz .Lq_Rg30\n"
      "s_branch .Lq_Cg30\n"
      ".Lq_Tg30:\n"
      "s_mov_b32 s69, 0\n"
      "s_branch .Lq_Cg30\n"
      ".Lq_Rg32:\n"
      "s_sub_u32 s69, s69, 1\n"
      "s_cbranch_scc1 .Lq_Tg32\n"
      "s_call_b64 s[2:3], .Lq_Lch32\n"
      "v_accvgpr_read_b32 v184, a128\n"
      "v_accvgpr_read_b32 v185, a132\n"
      "v_max_u32 v184, v184, v185\n"
      "v_cmp_eq_u32 vcc, -1, v184\n"
      "s_cbranch_vccnz .Lq_Rg32\n"
      "s_branch .Lq_Cg32\n"
      ".Lq_Tg32:\n"
      "s_mov_b32 s69, 0\n"
      "s_branch .Lq_Cg32\n"
      ".Lq_Lsk_g10:\n"
      "global_load_dwordx4 a[128:131], v253, s[80:81] offset:0 sc1\n"
      "v_mfma_f32_32x32x16_bf16 v[144:159], v[52:55], a[80:83], v[144:159]\n"
      "ds_read_b128 v[32:35], v255 offset:1024\n"
      "ds_read_b128 v[36:39], v255 offset:2048\n"
      "ds_read_b128 v[40:43], v255 offset:3072\n"
      "ds_read_b128 v[44:47], v255 offset:4096\n"
      "v_mfma_f32_32x32x16_bf16 v[128:143], v[56:59], a[84:87], v[128:143]\n"
      "global_load_dwordx4 a[132:135], v253, s[80:81] offset:1024 sc1\n"
      "s_waitcnt vmcnt(11)\n"
      "v_accvgpr_read_b32 v184, a88\n"
      "s_waitcnt vmcnt(10)\n"
      "v_accvgpr_read_b32 v185, a92\n"
      "v_max_u32 v184, v184, v185\n"
      "v_cmp_eq_u32 vcc, -1, v184\n"
      "v_mfma_f32_32x32x16_bf16 v[144:159], v[60:63], a[84:87], v[144:159]\n"
      "s_cbranch_vccnz .Lq_Rg22\n"
      ".Lq_Cg22:\n"
      "v_mfma_f32_32x32x16_bf16 v[128:143], v[64:67], a[88:91], v[128:143]\n"
      "global_load_dwordx4 a[136:139], v253, s[80:81] offset:2048 sc1\n"
      "ds_read_b128 v[48:51], v255 offset:5120\n"
      "ds_read_b128 v[52:55], v255 offset:6144\n"
      "ds_read_b128 v[56:59], v255 offset:7168\n"
      "ds_read_b128 v[60:63], v255 offset:33280\n"
      "v_mfma_f32_32x32x16_bf16 v[144:159], v[68:71], a[88:91], v[144:159]\n"
      "v_mfma_f32_32x32x16_bf16 v[128:143], v[72:75], a[92:95], v[128:143]\n"
      "global_load_dwordx4 a[140:143], v253, s[80:81] offset:3072 sc1\n"
      "s_waitcnt vmcnt(11)\n"
      "v_accvgpr_read_b32 v184, a96\n"
      "s_waitcnt vmcnt(10)\n"
      "v_accvgpr_read_b32 v185, a100\n"
      "v_max_u32 v184, v184, v185\n"
      "v_cmp_eq_u32 vcc, -1, v184\n"
      "v_mfma_f32_32x32x16_bf16 v[144:159], v[76:79], a[92:95], v[144:159]\n"
      "s_cbranch_vccnz .Lq_Rg24\n"
      ".Lq_Cg24:\n"
      "v_mfma_f32_32x32x16_bf16 v[128:143], v[80:83], a[96:99], v[128:143]\n"
      "global_load_dwordx4 a[144:147], v253, s[82:83] offset:0 sc1\n"
      "ds_read_b128 v[64:67], v255 offset:34304\n"
      "ds_read_b128 v[68:71], v255 offset:35328\n"
      "ds_read_b128 v[72:75], v255 offset:36352\n"
      "ds_read_b128 v[76:79], v255 offset:37376\n"
      "v_mfma_f32_32x32x16_bf16 v[144:159], v[84:87], a[96:99], v[144:159]\n"
      "v_mfma_f32_32x32x16_bf16 v[128:143], v[88:91], a[100:103], v[128:143]\n"
      "global_load_dwordx4 a[148:151], v253, s[82:83] offset:1024 sc1\n"
      "s_waitcnt vmcnt(11)\n"
      "v_accvgpr_read_b32 v184, a104\n"
      "s_waitcnt vmcnt(10)\n"
      "v_accvgpr_read_b32 v185, a108\n"
      "v_max_u32 v184, v184, v185\n"
      "v_cmp_eq_u32 vcc, -1, v184\n"
      "v_mfma_f32_32x32x16_bf16 v[144:159], v[92:95], a[100:103], v[144:159]\n"
      "s_cbranch_vccnz .Lq_Rg26\n"
      ".Lq_Cg26:\n"
      "v_mfma_f32_32x32x16_bf16 v[128:143], v[96:99], a[104:107], v[128:143]\n"
      "global_load_dwordx4 a[152:155], v253, s[82:83] offset:2048 sc1\n"
      "ds_read_b128 v[80:83], v255 offset:38400\n"
      "ds_read_b128 v[84:87], v255 offset:39424\n"
      "ds_read_b128 v[88:91], v255 offset:40448\n"
      "ds_read_b128 v[92:95], v223 offset:0\n"
      "v_mfma_f32_32x32x16_bf16 v[144:159], v[100:103], a[104:107], v[144:159]\n"
      "v_mfma_f32_32x32x16_bf16 v[128:143], v[104:107], a[108:111], v[128:143]\n"
      "global_load_dwordx4 a[156:159], v253, s[82:83] offset:3072 sc1\n"
      "s_waitcnt vmcnt(11)\n"
      "v_accvgpr_read_b32 v184, a112\n"
      "s_waitcnt vmcnt(10)\n"
      "v_accvgpr_read_b32 v185, a116\n"
      "v_max_u32 v184, v184, v185\n"
      "v_cmp_eq_u32 vcc, -1, v184\n"
      "v_mfma_f32_32x32x16_bf16 v[144:159], v[108:111], a[108:111], v[144:159]\n"
      "s_cbranch_vccnz .Lq_Rg28\n"
      ".Lq_Cg28:\n"
      "s_waitcnt lgkmcnt(15)\n"
      "v_mfma_f32_32x32x16_bf16 v[112:127], v[0:3], a[112:115], v[112:127]\n"
      "global_load_dwordx4 a[160:163], v253, s[84:85] offset:0 sc1\n"
      "ds_read_b128 v[96:99], v223 offset:1024\n"
      "ds_read_b128 v[100:103], v223 offset:2048\n"
      "ds_read_b128 v[104:107], v223 offset:3072\n"
      "ds_read_b128 v[108:111], v223 offset:4096\n"
      "v_mfma_f32_32x32x16_bf16 v[168:183], v[4:7], a[112:115], v[168:183]\n"
      "ds_read_b128 v[232:235], v220 offset:0\n"
      "ds_read_b128 v[236:239], v220 offset:1024\n"
      "v_mfma_f32_32x32x16_bf16 v[112:127], v[8:11], a[116:119], v[112:127]\n"
      "global_load_dwordx4 a[164:167], v253, s[84:85] offset:1024 sc1\n"
      "s_waitcnt vmcnt(11)\n"
      "v_accvgpr_read_b32 v184, a120\n"
      "s_waitcnt vmcnt(10)\n"
      "v_accvgpr_read_b32 v185, a124\n"
      "v_max_u32 v184, v184, v185\n"
      "v_cmp_eq_u32 vcc, -1, v184\n"
      "v_mfma_f32_32x32x16_bf16 v[168:183], v[12:15], a[116:119], v[168:183]\n"
      "s_cbranch_vccnz .Lq_Rg30\n"
      ".Lq_Cg30:\n"
      "v_mfma_f32_32x32x16_bf16 v[112:127], v[16:19], a[120:123], v[112:127]\n"
      "s_branch .Lq_Lsk_g15\n"
      ".Lq_Rg34:\n"
      "s_sub_u32 s69, s69, 1\n"
      "s_cbranch_scc1 .Lq_Tg34\n"
      "s_call_b64 s[2:3], .Lq_Lch34\n"
      "v_accvgpr_read_b32 v184, a136\n"
      "v_accvgpr_read_b32 v185, a140\n"
      "v_max_u32 v184, v184, v185\n"
      "v_cmp_eq_u32 vcc, -1, v184\n"
      "s_cbranch_vccnz .Lq_Rg34\n"
      "s_branch .Lq_Cg34\n"
      ".Lq_Tg34:\n"
      "s_mov_b32 s69, 0\n"
      "s_branch .Lq_Cg34\n"
      ".Lq_Rg36:\n"
      "s_sub_u32 s69, s69, 1\n"
      "s_cbranch_scc1 .Lq_Tg36\n"
      "s_call_b64 s[2:3], .Lq_Lch36\n"
      "v_accvgpr_read_b32 v184, a144\n"
      "v_accvgpr_read_b32 v185, a148\n"
      "v_max_u32 v184, v184, v185\n"
      "v_cmp_eq_u32 vcc, -1, v184\n"
      "s_cbranch_vccnz .Lq_Rg36\n"
      "s_branch .Lq_Cg36\n"
      ".Lq_Tg36:\n"
      "s_mov_b32 s69, 0\n"
      "s_branch .Lq_Cg36\n"
      ".Lq_Rg38:\n"
      "s_sub_u32 s69, s69, 1\n"
      "s_cbranch_scc1 .Lq_Tg38\n"
      "s_call_b64 s[2:3], .Lq_Lch38\n"
      "v_accvgpr_read_b32 v184, a152\n"
      "v_accvgpr_read_b32 v185, a156\n"
      "v_max_u32 v184, v184, v185\n"
      "v_cmp_eq_u32 vcc, -1, v184\n"
      "s_cbranch_vccnz .Lq_Rg38\n"
      "s_branch .Lq_Cg38\n"
      ".Lq_Tg38:\n"
      "s_mov_b32 s69, 0\n"
      "s_branch .Lq_Cg38\n"
      ".Lq_Rg40:\n"
      "s_sub_u32 s69, s69, 1\n"
      "s_cbranch_scc1 .Lq_Tg40\n"
      "s_call_b64 s[2:3], .Lq_Lch40\n"
      "v_accvgpr_read_b32 v184, a160\n"
      "v_accvgpr_read_b32 v185, a164\n"
      "v_max_u32 v184, v184, v185\n"
      "v_cmp_eq_u32 vcc, -1, v184\n"
      "s_cbranch_vccnz .Lq_Rg40\n"
      "s_branch .Lq_Cg40\n"
      ".Lq_Tg40:\n"
      "s_mov_b32 s69, 0\n"
      "s_branch .Lq_Cg40\n"
      ".Lq_Rg42:\n"
      "s_sub_u32 s69, s69, 1\n"
      "s_cbranch_scc1 .Lq_Tg42\n"
      "s_call_b64 s[2:3], .Lq_Lch42\n"
      "v_accvgpr_read_b32 v184, a168\n"
      "v_accvgpr_read_b32 v185, a172\n"
      "v_max_u32 v184, v184, v185\n"
      "v_cmp_eq_u32 vcc, -1, v184\n"
      "s_cbranch_vccnz .Lq_Rg42\n"
      "s_branch .Lq_Cg42\n"
      ".Lq_Tg42:\n"
      "s_mov_b32 s69, 0\n"
      "s_branch .Lq_Cg42\n"
      ".Lq_Lsk_g15:\n"
      "global_load_dwordx4 a[168:171], v254, s[52:53] offset:0 sc1\n"
      "v_mfma_f32_32x32x16_bf16 v[168:183], v[20:23], a[120:123], v[168:183]\n"
      "s_waitcnt lgkmcnt(0)\n"
      "s_barrier\n"
      "v_mfma_f32_32x32x16_bf16 v[112:127], v[24:27], a[124:127], v[112:127]\n"
      "global_load_dwordx4 a[172:175], v254, s[52:53] offset:1024 sc1\n"
      "s_waitcnt vmcnt(11)\n"
      "v_accvgpr_read_b32 v184, a128\n"
      "s_waitcnt vmcnt(10)\n"
      "v_accvgpr_read_b32 v185, a132\n"
      "v_max_u32 v184, v184, v185\n"
      "v_cmp_eq_u32 vcc, -1, v184\n"
      "v_mfma_f32_32x32x16_bf16 v[168:183], v[28:31], a[124:127], v[168:183]\n"
      "s_cbranch_vccnz .Lq_Rg32\n"
      ".Lq_Cg32:\n"
      "v_mfma_f32_32x32x16_bf16 v[112:127], v[32:35], a[128:131], v[112:127]\n"
      "global_load_dwordx4 a[176:179], v254, s[52:53] offset:2048 sc1\n"
      "v_mfma_f32_32x32x16_bf16 v[168:183], v[36:39], a[128:131], v[168:183]\n"
      "v_mfma_f32_32x32x16_bf16 v[112:127], v[40:43], a[132:135], v[112:127]\n"
      "global_load_dwordx4 a[180:183], v254, s[52:53] offset:3072 sc1\n"
      "s_waitcnt vmcnt(11)\n"
      "v_accvgpr_read_b32 v184, a136\n"
      "s_waitcnt vmcnt(10)\n"
      "v_accvgpr_read_b32 v185, a140\n"
      "v_max_u32 v184, v184, v185\n"
      "v_cmp_eq_u32 vcc, -1, v184\n"
      "v_mfma_f32_32x32x16_bf16 v[168:183], v[44:47], a[132:135], v[168:183]\n"
      "s_cbranch_vccnz .Lq_Rg34\n"
      ".Lq_Cg34:\n"
      "v_mfma_f32_32x32x16_bf16 v[112:127], v[48:51], a[136:139], v[112:127]\n"
      "global_load_dwordx4 a[184:187], v254, s[80:81] offset:0 sc1\n"
      "v_mfma_f32_32x32x16_bf16 v[168:183], v[52:55], a[136:139], v[168:183]\n"
      "v_mfma_f32_32x32x16_bf16 v[112:127], v[56:59], a[140:143], v[112:127]\n"
      "global_load_dwordx4 a[188:191], v254, s[80:81] offset:1024 sc1\n"
      "s_waitcnt vmcnt(11)\n"
      "v_accvgpr_read_b32 v184, a144\n"
      "s_waitcnt vmcnt(10)\n"
      "v_accvgpr_read_b32 v185, a148\n"
      "v_max_u32 v184, v184, v185\n"
      "v_cmp_eq_u32 vcc, -1, v184\n"
      "v_mfma_f32_32x32x16_bf16 v[168:183], v[60:63], a[140:143], v[168:183]\n"
      "s_cbranch_vccnz .Lq_Rg36\n"
      ".Lq_Cg36:\n"
      "v_mfma_f32_32x32x16_bf16 v[112:127], v[64:67], a[144:147], v[112:127]\n"
      "global_load_dwordx4 a[192:195], v254, s[80:81] offset:2048 sc1\n"
      "v_lshlrev_b32 v192, 16, v224\n"
      "v_and_b32 v196, 0xffff0000, v224\n"
      "v_lshlrev_b32 v193, 16, v225\n"
      "v_and_b32 v197, 0xffff0000, v225\n"
      "v_mfma_f32_32x32x16_bf16 v[168:183], v[68:71], a[144:147], v[168:183]\n"
      "v_lshlrev_b32 v194, 16, v226\n"
      "v_and_b32 v198, 0xffff0000, v226\n"
      "v_lshlrev_b32 v195, 16, v227\n"
      "v_and_b32 v199, 0xffff0000, v227\n"
      "v_lshlrev_b32 v200, 16, v228\n"
      "v_mfma_f32_32x32x16_bf16 v[112:127], v[72:75], a[148:151], v[112:127]\n"
      "global_load_dwordx4 a[196:199], v254, s[80:81] offset:3072 sc1\n"
      "s_waitcnt vmcnt(11)\n"
      "v_accvgpr_read_b32 v184, a152\n"
      "s_waitcnt vmcnt(10)\n"
      "v_accvgpr_read_b32 v185, a156\n"
      "v_max_u32 v184, v184, v185\n"
      "v_cmp_eq_u32 vcc, -1, v184\n"
      "v_mfma_f32_32x32x16_bf16 v[168:183], v[76:79], a[148:151], v[168:183]\n"
      "s_cbranch_vccnz .Lq_Rg38\n"
      ".Lq_Cg38:\n"
      "v_and_b32 v204, 0xffff0000, v228\n"
      "v_lshlrev_b32 v201, 16, v229\n"
      "v_and_b32 v205, 0xffff0000, v229\n"
      "v_lshlrev_b32 v202, 16, v230\n"
      "v_mfma_f32_32x32x16_bf16 v[112:127], v[80:83], a[152:155], v[112:127]\n"
      "global_load_dwordx4 a[200:203], v254, s[82:83] offset:0 sc1\n"
      "v_and_b32 v206, 0xffff0000, v230\n"
      "v_lshlrev_b32 v203, 16, v231\n"
      "v_and_b32 v207, 0xffff0000, v231\n"
      "v_mfma_f32_32x32x16_bf16 v[168:183], v[84:87], a[152:155], v[168:183]\n"
      "v_mfma_f32_32x32x16_bf16 v[112:127], v[88:91], a[156:159], v[112:127]\n"
      "global_load_dwordx4 a[204:207], v254, s[82:83] offset:1024 sc1\n"
      "s_waitcnt vmcnt(11)\n"
      "v_accvgpr_read_b32 v184, a160\n"
      "s_waitcnt vmcnt(10)\n"
      "v_accvgpr_read_b32 v185, a164\n"
      "v_max_u32 v184, v184, v185\n"
      "v_cmp_eq_u32 vcc, -1, v184\n"
      "v_mfma_f32_32x32x16_bf16 v[168:183], v[92:95], a[156:159], v[168:183]\n"
      "s_cbranch_vccnz .Lq_Rg40\n"
      ".Lq_Cg40:\n"
      "v_mfma_f32_32x32x16_bf16 v[112:127], v[96:99], a[160:163], v[112:127]\n"
      "s_branch .Lq_Lsk_g20\n"
      ".Lq_Rg44:\n"
      "s_sub_u32 s69, s69, 1\n"
      "s_cbranch_scc1 .Lq_Tg44\n"
      "s_call_b64 s[2:3], .Lq_Lch44\n"
      "v_accvgpr_read_b32 v184, a176\n"
      "v_accvgpr_read_b32 v185, a180\n"
      "v_max_u32 v184, v184, v185\n"
      "v_cmp_eq_u32 vcc, -1, v184\n"
      "s_cbranch_vccnz .Lq_Rg44\n"
      "s_branch .Lq_Cg44\n"
      ".Lq_Tg44:\n"
      "s_mov_b32 s69, 0\n"
      "s_branch .Lq_Cg44\n"
      ".Lq_Rg46:\n"
      "s_sub_u32 s69, s69, 1\n"
      "s_cbranch_scc1 .Lq_Tg46\n"
      "s_call_b64 s[2:3], .Lq_Lch46\n"
      "v_accvgpr_read_b32 v184, a184\n"
      "v_accvgpr_read_b32 v185, a188\n"
      "v_max_u32 v184, v184, v185\n"
      "v_cmp_eq_u32 vcc, -1, v184\n"
      "s_cbranch_vccnz .Lq_Rg46\n"
      "s_branch .Lq_Cg46\n"
      ".Lq_Tg46:\n"
      "s_mov_b32 s69, 0\n"
      "s_branch .Lq_Cg46\n"
      ".Lq_Rg48:\n"
      "s_sub_u32 s69, s69, 1\n"
      "s_cbranch_scc1 .Lq_Tg48\n"
      "s_call_b64 s[2:3], .Lq_Lch48\n"
      "v_accvgpr_read_b32 v184, a192\n"
      "v_accvgpr_read_b32 v185, a196\n"
      "v_max_u32 v184, v184, v185\n"
      "v_cmp_eq_u32 vcc, -1, v184\n"
      "s_cbranch_vccnz .Lq_Rg48\n"
      "s_branch .Lq_Cg48\n"
      ".Lq_Tg48:\n"
      "s_mov_b32 s69, 0\n"
      "s_branch .Lq_Cg48\n"
      ".Lq_Rg50:\n"
      "s_sub_u32 s69, s69, 1\n"
      "s_cbranch_scc1 .Lq_Tg50\n"
      "s_call_b64 s[2:3], .Lq_Lch50\n"
      "v_accvgpr_read_b32 v184, a200\n"
      "v_accvgpr_read_b32 v185, a204\n"
      "v_max_u32 v184, v184, v185\n"
      "v_cmp_eq_u32 vcc, -1, v184\n"
      "s_cbranch_vccnz .Lq_Rg50\n"
      "s_branch .Lq_Cg50\n"
      ".Lq_Tg50:\n"
      "s_mov_b32 s69, 0\n"
      "s_branch .Lq_Cg50\n"
      ".Lq_Rg52:\n"
      "s_sub_u32 s69, s69, 1\n"
      "s_cbranch_scc1 .Lq_Tg52\n"
      "s_call_b64 s[2:3], .Lq_Lch52\n"
      "v_accvgpr_read_b32 v184, a208\n"
      "v_accvgpr_read_b32 v185, a212\n"
      "v_max_u32 v184, v184, v185\n"
      "v_cmp_eq_u32 vcc, -1, v184\n"
      "s_cbranch_vccnz .Lq_Rg52\n"
      "s_branch .Lq_Cg52\n"
      ".Lq_Tg52:\n"
      "s_mov_b32 s69, 0\n"
      "s_branch .Lq_Cg52\n"
      ".Lq_Rg54:\n"
      "s_sub_u32 s69, s69, 1\n"
      "s_cbranch_scc1 .Lq_Tg54\n"
      "s_call_b64 s[2:3], .Lq_Lch54\n"
      "v_accvgpr_read_b32 v184, a216\n"
      "v_accvgpr_read_b32 v185, a220\n"
      "v_max_u32 v184, v184, v185\n"
      "v_cmp_eq_u32 vcc, -1, v184\n"
      "s_cbranch_vccnz .Lq_Rg54\n"
      "s_branch .Lq_Cg54\n"
      ".Lq_Tg54:\n"
      "s_mov_b32 s69, 0\n"
      "s_branch .Lq_Cg54\n"
      ".Lq_Lsk_g20:\n"
      "global_load_dwordx4 a[208:211], v254, s[82:83] offset:2048 sc1\n"
      "v_mfma_f32_32x32x16_bf16 v[168:183], v[100:103], a[160:163], v[168:183]\n"
      "v_mfma_f32_32x32x16_bf16 v[112:127], v[104:107], a[164:167], v[112:127]\n"
      "global_load_dwordx4 a[212:215], v254, s[82:83] offset:3072 sc1\n"
      "s_waitcnt vmcnt(11)\n"
      "v_accvgpr_read_b32 v184, a168\n"
      "s_waitcnt vmcnt(10)\n"
      "v_accvgpr_read_b32 v185, a172\n"
      "v_max_u32 v184, v184, v185\n"
      "v_cmp_eq_u32 vcc, -1, v184\n"
      "v_mfma_f32_32x32x16_bf16 v[168:183], v[108:111], a[164:167], v[168:183]\n"
      "s_cbranch_vccnz .Lq_Rg42\n"
      ".Lq_Cg42:\n"
      "v_mfma_f32_32x32x16_bf16 v[128:143], v[0:3], a[168:171], v[128:143]\n"
      "global_load_dwordx4 a[216:219], v254, s[84:85] offset:0 sc1\n"
      "v_mfma_f32_32x32x16_bf16 v[144:159], v[4:7], a[168:171], v[144:159]\n"
      "s_nop 1\n"
      "v_cmp_u_f32 vcc, v112, v112\n"
      "s_cbranch_vccnz .Lq_Ln0\n"
      ".Lq_Ln0_ret:\n"
      "ds_write_b128 v217, v[112:115] offset:0\n"
      "v_mfma_f32_32x32x16_bf16 v[128:143], v[8:11], a[172:175], v[128:143]\n"
      "global_load_dwordx4 a[220:223], v254, s[84:85] offset:1024 sc1\n"
      "s_waitcnt vmcnt(11)\n"
      "v_accvgpr_read_b32 v184, a176\n"
      "s_waitcnt vmcnt(10)\n"
      "v_accvgpr_read_b32 v185, a180\n"
      "v_max_u32 v184, v184, v185\n"
      "v_cmp_eq_u32 vcc, -1, v184\n"
      "ds_write_b128 v217, v[116:119] offset:1088\n"
      "v_mfma_f32_32x32x16_bf16 v[144:159], v[12:15], a[172:175], v[144:159]\n"
      "s_cbranch_vccnz .Lq_Rg44\n"
      ".Lq_Cg44:\n"
      "ds_write_b128 v217, v[120:123] offset:2176\n"
      "v_mfma_f32_32x32x16_bf16 v[128:143], v[16:19], a[176:179], v[128:143]\n"
      "ds_write_b128 v217, v[124:127] offset:3264\n"
      "v_mfma_f32_32x32x16_bf16 v[144:159], v[20:23], a[176:179], v[144:159]\n"
      "ds_write_b128 v217, v[168:171] offset:4480\n"
      "v_mfma_f32_32x32x16_bf16 v[128:143], v[24:27], a[180:183], v[128:143]\n"
      "s_waitcnt vmcnt(9)\n"
      "v_accvgpr_read_b32 v184, a184\n"
      "s_waitcnt vmcnt(8)\n"
      "v_accvgpr_read_b32 v185, a188\n"
      "v_max_u32 v184, v184, v185\n"
      "v_cmp_eq_u32 vcc, -1, v184\n"
      "ds_write_b128 v217, v[172:175] offset:5568\n"
      "v_mfma_f32_32x32x16_bf16 v[144:159], v[28:31], a[180:183], v[144:159]\n"
      "s_cbranch_vccnz .Lq_Rg46\n"
      ".Lq_Cg46:\n"
      "ds_write_b128 v217, v[176:179] offset:6656\n"
      "v_mfma_f32_32x32x16_bf16 v[128:143], v[32:35], a[184:187], v[128:143]\n"
      "ds_write_b128 v217, v[180:183] offset:7744\n"
      "v_mfma_f32_32x32x16_bf16 v[144:159], v[36:39], a[184:187], v[144:159]\n"
      "v_mfma_f32_32x32x16_bf16 v[128:143], v[40:43], a[188:191], v[128:143]\n"
      "s_waitcnt vmcnt(7)\n"
      "v_accvgpr_read_b32 v184, a192\n"
      "s_waitcnt vmcnt(6)\n"
      "v_accvgpr_read_b32 v185, a196\n"
      "v_max_u32 v184, v184, v185\n"
      "v_cmp_eq_u32 vcc, -1, v184\n"
      "v_mfma_f32_32x32x16_bf16 v[144:159], v[44:47], a[188:191], v[144:159]\n"
      "s_cbranch_vccnz .Lq_Rg48\n"
      ".Lq_Cg48:\n"
      "v_mfma_f32_32x32x16_bf16 v[128:143], v[48:51], a[192:195], v[128:143]\n"
      "s_waitcnt lgkmcnt(0)\n"
      "s_barrier\n"
      "ds_read_b128 v[112:115], v216 offset:0\n"
      "ds_read_b128 v[116:119], v216 offset:2176\n"
      "ds_read_b128 v[120:123], v216 offset:17920\n"
      "ds_read_b128 v[124:127], v216 offset:20096\n"
      "ds_read_b128 v[168:171], v216 offset:35840\n"
      "v_mfma_f32_32x32x16_bf16 v[144:159], v[52:55], a[192:195], v[144:159]\n"
      "ds_read_b128 v[172:175], v216 offset:38016\n"
      "ds_read_b128 v[176:179], v216 offset:53760\n"
      "ds_read_b128 v[180:183], v216 offset:55936\n"
      "v_mfma_f32_32x32x16_bf16 v[128:143], v[56:59], a[196:199], v[128:143]\n"
      "s_waitcnt vmcnt(5)\n"
      "v_accvgpr_read_b32 v184, a200\n"
      "s_waitcnt vmcnt(4)\n"
      "v_accvgpr_read_b32 v185, a204\n"
      "v_max_u32 v184, v184, v185\n"
      "v_cmp_eq_u32 vcc, -1, v184\n"
      "v_mfma_f32_32x32x16_bf16 v[144:159], v[60:63], a[196:199], v[144:159]\n"
      "s_cbranch_vccnz .Lq_Rg50\n"
      ".Lq_Cg50:\n"
      "s_waitcnt lgkmcnt(5)\n"
      "v_add_f32 v112, v112, v120\n"
      "v_add_f32 v113, v113, v121\n"
      "v_add_f32 v114, v114, v122\n"
      "v_add_f32 v115, v115, v123\n"
      "v_mfma_f32_32x32x16_bf16 v[128:143], v[64:67], a[200:203], v[128:143]\n"
      "s_waitcnt lgkmcnt(4)\n"
      "v_add_f32 v116, v116, v124\n"
      "v_add_f32 v117, v117, v125\n"
      "v_add_f32 v118, v118, v126\n"
      "v_add_f32 v119, v119, v127\n"
      "s_waitcnt lgkmcnt(1)\n"
      "v_add_f32 v168, v168, v176\n"
      "v_mfma_f32_32x32x16_bf16 v[144:159], v[68:71], a[200:203], v[144:159]\n"
      "v_add_f32 v169, v169, v177\n"
      "v_add_f32 v170, v170, v178\n"
      "v_add_f32 v171, v171, v179\n"
      "s_waitcnt lgkmcnt(0)\n"
      "v_add_f32 v172, v172, v180\n"
      "v_add_f32 v173, v173, v181\n"
      "v_mfma_f32_32x32x16_bf16 v[128:143], v[72:75], a[204:207], v[128:143]\n"
      "s_waitcnt vmcnt(3)\n"
      "v_accvgpr_read_b32 v184, a208\n"
      "s_waitcnt vmcnt(2)\n"
      "v_accvgpr_read_b32 v185, a212\n"
      "v_max_u32 v184, v184, v185\n"
      "v_cmp_eq_u32 vcc, -1, v184\n"
      "v_add_f32 v174, v174, v182\n"
      "v_mfma_f32_32x32x16_bf16 v[144:159], v[76:79], a[204:207], v[144:159]\n"
      "s_cbranch_vccnz .Lq_Rg52\n"
      ".Lq_Cg52:\n"
      "v_add_f32 v175, v175, v183\n"
      "v_add_f32 v112, v112, v168\n"
      "v_add_f32 v113, v113, v169\n"
      "v_add_f32 v114, v114, v170\n"
      "v_mfma_f32_32x32x16_bf16 v[128:143], v[80:83], a[208:211], v[128:143]\n"
      "v_add_f32 v115, v115, v171\n"
      "v_add_f32 v116, v116, v172\n"
      "v_add_f32 v117, v117, v173\n"
      "v_add_f32 v118, v118, v174\n"
      "v_add_f32 v119, v119, v175\n"
      "v_mfma_f32_32x32x16_bf16 v[144:159], v[84:87], a[208:211], v[144:159]\n"
      "v_fmac_f32 v192, v112, v232\n"
      "v_fmac_f32 v193, v113, v233\n"
      "v_fmac_f32 v194, v114, v234\n"
      "v_fmac_f32 v195, v115, v235\n"
      "v_fmac_f32 v196, v116, v236\n"
      "v_mfma_f32_32x32x16_bf16 v[128:143], v[88:91], a[212:215], v[128:143]\n"
      "s_waitcnt vmcnt(1)\n"
      "v_accvgpr_read_b32 v184, a216\n"
      "s_waitcnt vmcnt(0)\n"
      "v_accvgpr_read_b32 v185, a220\n"
      "v_max_u32 v184, v184, v185\n"
      "v_cmp_eq_u32 vcc, -1, v184\n"
      "v_fmac_f32 v197, v117, v237\n"
      "v_mfma_f32_32x32x16_bf16 v[144:159], v[92:95], a[212:215], v[144:159]\n"
      "s_cbranch_vccnz .Lq_Rg54\n"
      ".Lq_Cg54:\n"
      "v_fmac_f32 v198, v118, v238\n"
      "v_fmac_f32 v199, v119, v239\n"
      "v_cvt_pk_bf16_f32 v224, v192, v196\n"
      "v_cvt_pk_bf16_f32 v225, v193, v197\n"
      "v_mfma_f32_32x32x16_bf16 v[128:143], v[96:99], a[216:219], v[128:143]\n"
      "v_cvt_pk_bf16_f32 v226, v194, v198\n"
      "v_cvt_pk_bf16_f32 v227, v195, v199\n"
      "global_store_dwordx4 v221, v[224:227], s[54:55] offset:0\n"
      "v_mfma_f32_32x32x16_bf16 v[144:159], v[100:103], a[216:219], v[144:159]\n"
      "v_mfma_f32_32x32x16_bf16 v[128:143], v[104:107], a[220:223], v[128:143]\n"
      "v_mfma_f32_32x32x16_bf16 v[144:159], v[108:111], a[220:223], v[144:159]\n"
      "s_nop 10\n"
      "v_cmp_u_f32 vcc, v128, v128\n"
      "s_cbranch_vccnz .Lq_Ln1\n"
      ".Lq_Ln1_ret:\n"
      "ds_write_b128 v217, v[128:131] offset:8960\n"
      "ds_write_b128 v217, v[132:135] offset:10048\n"
      "ds_write_b128 v217, v[136:139] offset:11136\n"
      "ds_write_b128 v217, v[140:143] offset:12224\n"
      "ds_write_b128 v217, v[144:147] offset:13440\n"
      "ds_write_b128 v217, v[148:151] offset:14528\n"
      "ds_write_b128 v217, v[152:155] offset:15616\n"
      "ds_write_b128 v217, v[156:159] offset:16704\n"
      "s_waitcnt lgkmcnt(0)\n"
      "s_barrier\n"
      "ds_read_b128 v[112:115], v216 offset:8960\n"
      "ds_read_b128 v[116:119], v216 offset:11136\n"
      "ds_read_b128 v[120:123], v216 offset:26880\n"
      "ds_read_b128 v[124:127], v216 offset:29056\n"
      "ds_read_b128 v[168:171], v216 offset:44800\n"
      "ds_read_b128 v[172:175], v216 offset:46976\n"
      "ds_read_b128 v[176:179], v216 offset:62720\n"
      "ds_read_b128 v[180:183], v216 offset:64896\n"
      "s_waitcnt lgkmcnt(5)\n"
      "v_pk_add_f32 v[112:113], v[112:113], v[120:121]\n"
      "v_pk_add_f32 v[114:115], v[114:115], v[122:123]\n"
      "s_waitcnt lgkmcnt(4)\n"
      "v_pk_add_f32 v[116:117], v[116:117], v[124:125]\n"
      "v_pk_add_f32 v[118:119], v[118:119], v[126:127]\n"
      "s_waitcnt lgkmcnt(1)\n"
      "v_pk_add_f32 v[168:169], v[168:169], v[176:177]\n"
      "v_pk_add_f32 v[170:171], v[170:171], v[178:179]\n"
      "s_waitcnt lgkmcnt(0)\n"
      "v_pk_add_f32 v[172:173], v[172:173], v[180:181]\n"
      "v_pk_add_f32 v[174:175], v[174:175], v[182:183]\n"
      "v_pk_add_f32 v[112:113], v[112:113], v[168:169]\n"
      "v_pk_add_f32 v[114:115], v[114:115], v[170:171]\n"
      "v_pk_add_f32 v[116:117], v[116:117], v[172:173]\n"
      "v_pk_add_f32 v[118:119], v[118:119], v[174:175]\n"
      "v_pk_fma_f32 v[200:201], v[112:113], v[232:233], v[200:201]\n"
      "v_pk_fma_f32 v[202:203], v[114:115], v[234:235], v[202:203]\n"
      "v_pk_fma_f32 v[204:205], v[116:117], v[236:237], v[204:205]\n"
      "v_pk_fma_f32 v[206:207], v[118:119], v[238:239], v[206:207]\n"
      "v_cvt_pk_bf16_f32 v228, v200, v204\n"
      "v_cvt_pk_bf16_f32 v229, v201, v205\n"
      "v_cvt_pk_bf16_f32 v230, v202, v206\n"
      "v_cvt_pk_bf16_f32 v231, v203, v207\n"
      "global_store_dwordx4 v221, v[228:231], s[54:55] offset:1024\n"
      "s_endpgm\n"
      ".Lq_Rh1:\n"
      "s_sub_u32 s69, s69, 1\n"
      "s_cbranch_scc1 .Lq_Th1\n"
      "s_nop 7\n"
      "s_waitcnt lgkmcnt(0)\n"
      "s_add_u32 m0, s61, 33280\n"
      "s_nop 0\n"
      "global_load_lds_dwordx4 v240, s[18:19] offset:0 sc1\n"
      "s_add_u32 m0, s61, 33296\n"
      "s_nop 0\n"
      "global_load_lds_dwordx4 v240, s[18:19] offset:1024 sc1\n"
      "s_add_u32 m0, s61, 33312\n"
      "s_nop 0\n"
      "global_load_lds_dwordx4 v240, s[18:19] offset:2048 sc1\n"
      "s_add_u32 m0, s61, 33328\n"
      "s_nop 0\n"
      "global_load_lds_dwordx4 v240, s[18:19] offset:3072 sc1\n"
      "s_add_u32 m0, s61, 37440\n"
      "s_nop 0\n"
      "global_load_lds_dwordx4 v241, s[18:19] offset:0 sc1\n"
      "s_add_u32 m0, s61, 37456\n"
      "s_nop 0\n"
      "global_load_lds_dwordx4 v241, s[18:19] offset:1024 sc1\n"
      "s_add_u32 m0, s61, 37472\n"
      "s_nop 0\n"
      "global_load_lds_dwordx4 v241, s[18:19] offset:2048 sc1\n"
      "s_add_u32 m0, s61, 37488\n"
      "s_nop 0\n"
      "global_load_lds_dwordx4 v241, s[18:19] offset:3072 sc1\n"
      "s_waitcnt vmcnt(0)\n"
      "ds_read_b128 v[80:83], v244 offset:33280\n"
      "ds_read_b128 v[84:87], v244 offset:34320\n"
      "ds_read_b128 v[88:91], v244 offset:35360\n"
      "ds_read_b128 v[92:95], v244 offset:36400\n"
      "s_waitcnt lgkmcnt(3)\n"
      "v_max3_u32 v200, v80, v81, v82\n"
      "v_mov_b32 v201, v83\n"
      "s_waitcnt lgkmcnt(2)\n"
      "v_max3_u32 v200, v200, v84, v85\n"
      "v_max3_u32 v201, v201, v86, v87\n"
      "s_waitcnt lgkmcnt(1)\n"
      "v_max3_u32 v200, v200, v88, v89\n"
      "v_max3_u32 v201, v201, v90, v91\n"
      "s_waitcnt lgkmcnt(0)\n"
      "v_max3_u32 v200, v200, v92, v93\n"
      "v_max3_u32 v201, v201, v94, v95\n"
      "ds_read_b128 v[80:83], v244 offset:37440\n"
      "ds_read_b128 v[84:87], v244 offset:38480\n"
      "ds_read_b128 v[88:91], v244 offset:39520\n"
      "ds_read_b128 v[92:95], v244 offset:40560\n"
      "s_waitcnt lgkmcnt(3)\n"
      "v_max3_u32 v200, v200, v80, v81\n"
      "v_max3_u32 v201, v201, v82, v83\n"
      "s_waitcnt lgkmcnt(2)\n"
      "v_max3_u32 v200, v200, v84, v85\n"
      "v_max3_u32 v201, v201, v86, v87\n"
      "s_waitcnt lgkmcnt(1)\n"
      "v_max3_u32 v200, v200, v88, v89\n"
      "v_max3_u32 v201, v201, v90, v91\n"
      "s_waitcnt lgkmcnt(0)\n"
      "v_max3_u32 v200, v200, v92, v93\n"
      "v_max3_u32 v201, v201, v94, v95\n"
      "v_max_u32 v200, v200, v201\n"
      "v_cmp_eq_u32 vcc, -1, v200\n"
      "s_cbranch_vccnz .Lq_Rh1\n"
      "s_branch .Lq_Ch1\n"
      ".Lq_Th1:\n"
      "s_mov_b32 s69, 0\n"
      "s_branch .Lq_Ch1\n"
      ".Lq_Rh2:\n"
      "s_sub_u32 s69, s69, 1\n"
      "s_cbranch_scc1 .Lq_Th2\n"
      "s_nop 7\n"
      "s_waitcnt lgkmcnt(0)\n"
      "s_add_u32 m0, s61, 66560\n"
      "s_nop 0\n"
      "global_load_lds_dwordx4 v240, s[20:21] offset:0 sc1\n"
      "s_add_u32 m0, s61, 66576\n"
      "s_nop 0\n"
      "global_load_lds_dwordx4 v240, s[20:21] offset:1024 sc1\n"
      "s_add_u32 m0, s61, 66592\n"
      "s_nop 0\n"
      "global_load_lds_dwordx4 v240, s[20:21] offset:2048 sc1\n"
      "s_add_u32 m0, s61, 66608\n"
      "s_nop 0\n"
      "global_load_lds_dwordx4 v240, s[20:21] offset:3072 sc1\n"
      "s_add_u32 m0, s61, 70720\n"
      "s_nop 0\n"
      "global_load_lds_dwordx4 v241, s[20:21] offset:0 sc1\n"
      "s_add_u32 m0, s61, 70736\n"
      "s_nop 0\n"
      "global_load_lds_dwordx4 v241, s[20:21] offset:1024 sc1\n"
      "s_add_u32 m0, s61, 70752\n"
      "s_nop 0\n"
      "global_load_lds_dwordx4 v241, s[20:21] offset:2048 sc1\n"
      "s_add_u32 m0, s61, 70768\n"
      "s_nop 0\n"
      "global_load_lds_dwordx4 v241, s[20:21] offset:3072 sc1\n"
      "s_waitcnt vmcnt(0)\n"
      "ds_read_b128 v[224:227], v244 offset:0\n"
      "ds_read_b128 v[228:231], v244 offset:1040\n"
      "ds_read_b128 v[232:235], v244 offset:2080\n"
      "ds_read_b128 v[236:239], v244 offset:3120\n"
      "s_waitcnt lgkmcnt(3)\n"
      "v_max3_u32 v200, v224, v225, v226\n"
      "v_mov_b32 v201, v227\n"
      "s_waitcnt lgkmcnt(2)\n"
      "v_max3_u32 v200, v200, v228, v229\n"
      "v_max3_u32 v201, v201, v230, v231\n"
      "s_waitcnt lgkmcnt(1)\n"
      "v_max3_u32 v200, v200, v232, v233\n"
      "v_max3_u32 v201, v201, v234, v235\n"
      "s_waitcnt lgkmcnt(0)\n"
      "v_max3_u32 v200, v200, v236, v237\n"
      "v_max3_u32 v201, v201, v238, v239\n"
      "ds_read_b128 v[224:227], v244 offset:4160\n"
      "ds_read_b128 v[228:231], v244 offset:5200\n"
      "ds_read_b128 v[232:235], v244 offset:6240\n"
      "ds_read_b128 v[236:239], v244 offset:7280\n"
      "s_waitcnt lgkmcnt(3)\n"
      "v_max3_u32 v200, v200, v224, v225\n"
      "v_max3_u32 v201, v201, v226, v227\n"
      "s_waitcnt lgkmcnt(2)\n"
      "v_max3_u32 v200, v200, v228, v229\n"
      "v_max3_u32 v201, v201, v230, v231\n"
      "s_waitcnt lgkmcnt(1)\n"
      "v_max3_u32 v200, v200, v232, v233\n"
      "v_max3_u32 v201, v201, v234, v235\n"
      "s_waitcnt lgkmcnt(0)\n"
      "v_max3_u32 v200, v200, v236, v237\n"
      "v_max3_u32 v201, v201, v238, v239\n"
      "v_max_u32 v200, v200, v201\n"
      "v_cmp_eq_u32 vcc, -1, v200\n"
      "s_cbranch_vccnz .Lq_Rh2\n"
      "s_branch .Lq_Ch2\n"
      ".Lq_Th2:\n"
      "s_mov_b32 s69, 0\n"
      "s_branch .Lq_Ch2\n"
      ".Lq_Rh3:\n"
      "s_sub_u32 s69, s69, 1\n"
      "s_cbranch_scc1 .Lq_Th3\n"
      "s_nop 7\n"
      "s_waitcnt lgkmcnt(0)\n"
      "s_add_u32 m0, s61, 99840\n"
      "s_nop 0\n"
      "global_load_lds_dwordx4 v240, s[22:23] offset:0 sc1\n"
      "s_add_u32 m0, s61, 99856\n"
      "s_nop 0\n"
      "global_load_lds_dwordx4 v240, s[22:23] offset:1024 sc1\n"
      "s_add_u32 m0, s61, 99872\n"
      "s_nop 0\n"
      "global_load_lds_dwordx4 v240, s[22:23] offset:2048 sc1\n"
      "s_add_u32 m0, s61, 99888\n"
      "s_nop 0\n"
      "global_load_lds_dwordx4 v240, s[22:23] offset:3072 sc1\n"
      "s_add_u32 m0, s61, 104000\n"
      "s_nop 0\n"
      "global_load_lds_dwordx4 v241, s[22:23] offset:0 sc1\n"
      "s_add_u32 m0, s61, 104016\n"
      "s_nop 0\n"
      "global_load_lds_dwordx4 v241, s[22:23] offset:1024 sc1\n"
      "s_add_u32 m0, s61, 104032\n"
      "s_nop 0\n"
      "global_load_lds_dwordx4 v241, s[22:23] offset:2048 sc1\n"
      "s_add_u32 m0, s61, 104048\n"
      "s_nop 0\n"
      "global_load_lds_dwordx4 v241, s[22:23] offset:3072 sc1\n"
      "s_waitcnt vmcnt(0)\n"
      "ds_read_b128 v[224:227], v244 offset:33280\n"
      "ds_read_b128 v[228:231], v244 offset:34320\n"
      "ds_read_b128 v[232:235], v244 offset:35360\n"
      "ds_read_b128 v[236:239], v244 offset:36400\n"
      "s_waitcnt lgkmcnt(3)\n"
      "v_max3_u32 v200, v224, v225, v226\n"
      "v_mov_b32 v201, v227\n"
      "s_waitcnt lgkmcnt(2)\n"
      "v_max3_u32 v200, v200, v228, v229\n"
      "v_max3_u32 v201, v201, v230, v231\n"
      "s_waitcnt lgkmcnt(1)\n"
      "v_max3_u32 v200, v200, v232, v233\n"
      "v_max3_u32 v201, v201, v234, v235\n"
      "s_waitcnt lgkmcnt(0)\n"
      "v_max3_u32 v200, v200, v236, v237\n"
      "v_max3_u32 v201, v201, v238, v239\n"
      "ds_read_b128 v[224:227], v244 offset:37440\n"
      "ds_read_b128 v[228:231], v244 offset:38480\n"
      "ds_read_b128 v[232:235], v244 offset:39520\n"
      "ds_read_b128 v[236:239], v244 offset:40560\n"
      "s_waitcnt lgkmcnt(3)\n"
      "v_max3_u32 v200, v200, v224, v225\n"
      "v_max3_u32 v201, v201, v226, v227\n"
      "s_waitcnt lgkmcnt(2)\n"
      "v_max3_u32 v200, v200, v228, v229\n"
      "v_max3_u32 v201, v201, v230, v231\n"
      "s_waitcnt lgkmcnt(1)\n"
      "v_max3_u32 v200, v200, v232, v233\n"
      "v_max3_u32 v201, v201, v234, v235\n"
      "s_waitcnt lgkmcnt(0)\n"
      "v_max3_u32 v200, v200, v236, v237\n"
      "v_max3_u32 v201, v201, v238, v239\n"
      "v_max_u32 v200, v200, v201\n"
      "v_cmp_eq_u32 vcc, -1, v200\n"
      "s_cbranch_vccnz .Lq_Rh3\n"
      "s_branch .Lq_Ch3\n"
      ".Lq_Th3:\n"
      "s_mov_b32 s69, 0\n"
      "s_branch .Lq_Ch3\n"
      ".Lq_RF0:\n"
      "s_sub_u32 s69, s69, 1\n"
      "s_cbranch_scc1 .Lq_TF0\n"
      "s_mov_b64 exec, vcc\n"
      "global_load_dword v232, v212, s[52:53] offset:0 sc1\n"
      "s_mov_b64 exec, -1\n"
      "s_waitcnt vmcnt(0)\n"
      "v_cmp_ne_u32 vcc, s67, v232\n"
      "s_cbranch_vccnz .Lq_RF0\n"
      "s_branch .Lq_CF0\n"
      ".Lq_TF0:\n"
      "s_mov_b32 s69, 0\n"
      "s_branch .Lq_CF0\n"
      ".Lq_Lch14:\n"
      "global_load_dwordx4 a[56:59], v252, s[52:53] offset:0 sc1\n"
      "global_load_dwordx4 a[60:63], v252, s[52:53] offset:1024 sc1\n"
      ".Lq_Lch16:\n"
      "global_load_dwordx4 a[64:67], v252, s[52:53] offset:2048 sc1\n"
      "global_load_dwordx4 a[68:71], v252, s[52:53] offset:3072 sc1\n"
      ".Lq_Lch18:\n"
      "global_load_dwordx4 a[72:75], v252, s[80:81] offset:0 sc1\n"
      "global_load_dwordx4 a[76:79], v252, s[80:81] offset:1024 sc1\n"
      ".Lq_Lch20:\n"
      "global_load_dwordx4 a[80:83], v252, s[80:81] offset:2048 sc1\n"
      "global_load_dwordx4 a[84:87], v252, s[80:81] offset:3072 sc1\n"
      ".Lq_Lch22:\n"
      "global_load_dwordx4 a[88:91], v252, s[82:83] offset:0 sc1\n"
      "global_load_dwordx4 a[92:95], v252, s[82:83] offset:1024 sc1\n"
      ".Lq_Lch24:\n"
      "global_load_dwordx4 a[96:99], v252, s[82:83] offset:2048 sc1\n"
      "global_load_dwordx4 a[100:103], v252, s[82:83] offset:3072 sc1\n"
      ".Lq_Lch26:\n"
      "global_load_dwordx4 a[104:107], v252, s[84:85] offset:0 sc1\n"
      "global_load_dwordx4 a[108:111], v252, s[84:85] offset:1024 sc1\n"
      "s_waitcnt vmcnt(0)\n"
      "s_setpc_b64 s[2:3]\n"
      ".Lq_Lch28:\n"
      "global_load_dwordx4 a[112:115], v253, s[52:53] offset:0 sc1\n"
      "global_load_dwordx4 a[116:119], v253, s[52:53] offset:1024 sc1\n"
      ".Lq_Lch30:\n"
      "global_load_dwordx4 a[120:123], v253, s[52:53] offset:2048 sc1\n"
      "global_load_dwordx4 a[124:127], v253, s[52:53] offset:3072 sc1\n"
      ".Lq_Lch32:\n"
      "global_load_dwordx4 a[128:131], v253, s[80:81] offset:0 sc1\n"
      "global_load_dwordx4 a[132:135], v253, s[80:81] offset:1024 sc1\n"
      ".Lq_Lch34:\n"
      "global_load_dwordx4 a[136:139], v253, s[80:81] offset:2048 sc1\n"
      "global_load_dwordx4 a[140:143], v253, s[80:81] offset:3072 sc1\n"
      ".Lq_Lch36:\n"
      "global_load_dwordx4 a[144:147], v253, s[82:83] offset:0 sc1\n"
      "global_load_dwordx4 a[148:151], v253, s[82:83] offset:1024 sc1\n"
      ".Lq_Lch38:\n"
      "global_load_dwordx4 a[152:155], v253, s[82:83] offset:2048 sc1\n"
      "global_load_dwordx4 a[156:159], v253, s[82:83] offset:3072 sc1\n"
      ".Lq_Lch40:\n"
      "global_load_dwordx4 a[160:163], v253, s[84:85] offset:0 sc1\n"
      "global_load_dwordx4 a[164:167], v253, s[84:85] offset:1024 sc1\n"
      "s_waitcnt vmcnt(0)\n"
      "s_setpc_b64 s[2:3]\n"
      ".Lq_Lch42:\n"
      "global_load_dwordx4 a[168:171], v254, s[52:53] offset:0 sc1\n"
      "global_load_dwordx4 a[172:175], v254, s[52:53] offset:1024 sc1\n"
      ".Lq_Lch44:\n"
      "global_load_dwordx4 a[176:179], v254, s[52:53] offset:2048 sc1\n"
      "global_load_dwordx4 a[180:183], v254, s[52:53] offset:3072 sc1\n"
      ".Lq_Lch46:\n"
      "global_load_dwordx4 a[184:187], v254, s[80:81] offset:0 sc1\n"
      "global_load_dwordx4 a[188:191], v254, s[80:81] offset:1024 sc1\n"
      ".Lq_Lch48:\n"
      "global_load_dwordx4 a[192:195], v254, s[80:81] offset:2048 sc1\n"
      "global_load_dwordx4 a[196:199], v254, s[80:81] offset:3072 sc1\n"
      ".Lq_Lch50:\n"
      "global_load_dwordx4 a[200:203], v254, s[82:83] offset:0 sc1\n"
      "global_load_dwordx4 a[204:207], v254, s[82:83] offset:1024 sc1\n"
      ".Lq_Lch52:\n"
      "global_load_dwordx4 a[208:211], v254, s[82:83] offset:2048 sc1\n"
      "global_load_dwordx4 a[212:215], v254, s[82:83] offset:3072 sc1\n"
      ".Lq_Lch54:\n"
      "global_load_dwordx4 a[216:219], v254, s[84:85] offset:0 sc1\n"
      "global_load_dwordx4 a[220:223], v254, s[84:85] offset:1024 sc1\n"
      "s_waitcnt vmcnt(0)\n"
      "s_setpc_b64 s[2:3]\n"
      ".Lq_Ln0:\n"
      "s_nop 15\n"
      "v_mov_b32 v112, 0\n"
      "v_mov_b32 v113, 0\n"
      "v_mov_b32 v114, 0\n"
      "v_mov_b32 v115, 0\n"
      "v_mov_b32 v116, 0\n"
      "v_mov_b32 v117, 0\n"
      "v_mov_b32 v118, 0\n"
      "v_mov_b32 v119, 0\n"
      "v_mov_b32 v120, 0\n"
      "v_mov_b32 v121, 0\n"
      "v_mov_b32 v122, 0\n"
      "v_mov_b32 v123, 0\n"
      "v_mov_b32 v124, 0\n"
      "v_mov_b32 v125, 0\n"
      "v_mov_b32 v126, 0\n"
      "v_mov_b32 v127, 0\n"
      "v_mov_b32 v168, 0\n"
      "v_mov_b32 v169, 0\n"
      "v_mov_b32 v170, 0\n"
      "v_mov_b32 v171, 0\n"
      "v_mov_b32 v172, 0\n"
      "v_mov_b32 v173, 0\n"
      "v_mov_b32 v174, 0\n"
      "v_mov_b32 v175, 0\n"
      "v_mov_b32 v176, 0\n"
      "v_mov_b32 v177, 0\n"
      "v_mov_b32 v178, 0\n"
      "v_mov_b32 v179, 0\n"
      "v_mov_b32 v180, 0\n"
      "v_mov_b32 v181, 0\n"
      "v_mov_b32 v182, 0\n"
      "v_mov_b32 v183, 0\n"
      "s_mov_b64 s[4:5], s[52:53]\n"
      "s_movk_i32 s6, 14\n"
      ".Lq_Ln0_0:\n"
      "global_load_dwordx4 v[188:191], v251, s[4:5] offset:0 sc1\n"
      "global_load_dwordx4 v[160:163], v247, s[4:5] offset:0 sc1\n"
      "global_load_dwordx4 v[164:167], v248, s[4:5] offset:0 sc1\n"
      "s_waitcnt vmcnt(0)\n"
      "v_max3_u32 v186, v188, v189, v190\n"
      "v_max_u32 v186, v186, v191\n"
      "v_cmp_eq_u32 vcc, -1, v186\n"
      "s_cbranch_vccz .Lq_Lm0_0\n"
      "s_sub_u32 s69, s69, 1\n"
      "s_cbranch_scc0 .Lq_Ln0_0\n"
      "s_mov_b32 s69, 0\n"
      ".Lq_Lm0_0:\n"
      "v_mfma_f32_32x32x16_bf16 v[112:127], v[160:163], v[188:191], v[112:127]\n"
      "v_mfma_f32_32x32x16_bf16 v[168:183], v[164:167], v[188:191], v[168:183]\n"
      "s_add_u32 s4, s4, 1024\n"
      "s_addc_u32 s5, s5, 0\n"
      "s_sub_u32 s6, s6, 1\n"
      "s_cmp_lg_u32 s6, 0\n"
      "s_cbranch_scc1 .Lq_Ln0_0\n"
      "s_mov_b64 s[4:5], s[52:53]\n"
      "s_movk_i32 s6, 14\n"
      ".Lq_Ln0_1:\n"
      "global_load_dwordx4 v[188:191], v253, s[4:5] offset:0 sc1\n"
      "global_load_dwordx4 v[160:163], v249, s[4:5] offset:0 sc1\n"
      "global_load_dwordx4 v[164:167], v250, s[4:5] offset:0 sc1\n"
      "s_waitcnt vmcnt(0)\n"
      "v_max3_u32 v186, v188, v189, v190\n"
      "v_max_u32 v186, v186, v191\n"
      "v_cmp_eq_u32 vcc, -1, v186\n"
      "s_cbranch_vccz .Lq_Lm0_1\n"
      "s_sub_u32 s69, s69, 1\n"
      "s_cbranch_scc0 .Lq_Ln0_1\n"
      "s_mov_b32 s69, 0\n"
      ".Lq_Lm0_1:\n"
      "v_mfma_f32_32x32x16_bf16 v[112:127], v[160:163], v[188:191], v[112:127]\n"
      "v_mfma_f32_32x32x16_bf16 v[168:183], v[164:167], v[188:191], v[168:183]\n"
      "s_add_u32 s4, s4, 1024\n"
      "s_addc_u32 s5, s5, 0\n"
      "s_sub_u32 s6, s6, 1\n"
      "s_cmp_lg_u32 s6, 0\n"
      "s_cbranch_scc1 .Lq_Ln0_1\n"
      "s_nop 15\n"
      "s_branch .Lq_Ln0_ret\n"
      ".Lq_Ln1:\n"
      "s_nop 15\n"
      "v_mov_b32 v128, 0\n"
      "v_mov_b32 v129, 0\n"
      "v_mov_b32 v130, 0\n"
      "v_mov_b32 v131, 0\n"
      "v_mov_b32 v132, 0\n"
      "v_mov_b32 v133, 0\n"
      "v_mov_b32 v134, 0\n"
      "v_mov_b32 v135, 0\n"
      "v_mov_b32 v136, 0\n"
      "v_mov_b32 v137, 0\n"
      "v_mov_b32 v138, 0\n"
      "v_mov_b32 v139, 0\n"
      "v_mov_b32 v140, 0\n"
      "v_mov_b32 v141, 0\n"
      "v_mov_b32 v142, 0\n"
      "v_mov_b32 v143, 0\n"
      "v_mov_b32 v144, 0\n"
      "v_mov_b32 v145, 0\n"
      "v_mov_b32 v146, 0\n"
      "v_mov_b32 v147, 0\n"
      "v_mov_b32 v148, 0\n"
      "v_mov_b32 v149, 0\n"
      "v_mov_b32 v150, 0\n"
      "v_mov_b32 v151, 0\n"
      "v_mov_b32 v152, 0\n"
      "v_mov_b32 v153, 0\n"
      "v_mov_b32 v154, 0\n"
      "v_mov_b32 v155, 0\n"
      "v_mov_b32 v156, 0\n"
      "v_mov_b32 v157, 0\n"
      "v_mov_b32 v158, 0\n"
      "v_mov_b32 v159, 0\n"
      "s_mov_b64 s[4:5], s[52:53]\n"
      "s_movk_i32 s6, 14\n"
      ".Lq_Ln1_0:\n"
      "global_load_dwordx4 v[188:191], v252, s[4:5] offset:0 sc1\n"
      "global_load_dwordx4 v[160:163], v247, s[4:5] offset:0 sc1\n"
      "global_load_dwordx4 v[164:167], v248, s[4:5] offset:0 sc1\n"
      "s_waitcnt vmcnt(0)\n"
      "v_max3_u32 v186, v188, v189, v190\n"
      "v_max_u32 v186, v186, v191\n"
      "v_cmp_eq_u32 vcc, -1, v186\n"
      "s_cbranch_vccz .Lq_Lm1_0\n"
      "s_sub_u32 s69, s69, 1\n"
      "s_cbranch_scc0 .Lq_Ln1_0\n"
      "s_mov_b32 s69, 0\n"
      ".Lq_Lm1_0:\n"
      "v_mfma_f32_32x32x16_bf16 v[128:143], v[160:163], v[188:191], v[128:143]\n"
      "v_mfma_f32_32x32x16_bf16 v[144:159], v[164:167], v[188:191], v[144:159]\n"
      "s_add_u32 s4, s4, 1024\n"
      "s_addc_u32 s5, s5, 0\n"
      "s_sub_u32 s6, s6, 1\n"
      "s_cmp_lg_u32 s6, 0\n"
      "s_cbranch_scc1 .Lq_Ln1_0\n"
      "s_mov_b64 s[4:5], s[52:53]\n"
      "s_movk_i32 s6, 14\n"
      ".Lq_Ln1_1:\n"
      "global_load_dwordx4 v[188:191], v254, s[4:5] offset:0 sc1\n"
      "global_load_dwordx4 v[160:163], v249, s[4:5] offset:0 sc1\n"
      "global_load_dwordx4 v[164:167], v250, s[4:5] offset:0 sc1\n"
      "s_waitcnt vmcnt(0)\n"
      "v_max3_u32 v186, v188, v189, v190\n"
      "v_max_u32 v186, v186, v191\n"
      "v_cmp_eq_u32 vcc, -1, v186\n"
      "s_cbranch_vccz .Lq_Lm1_1\n"
      "s_sub_u32 s69, s69, 1\n"
      "s_cbranch_scc0 .Lq_Ln1_1\n"
      "s_mov_b32 s69, 0\n"
      ".Lq_Lm1_1:\n"
      "v_mfma_f32_32x32x16_bf16 v[128:143], v[160:163], v[188:191], v[128:143]\n"
      "v_mfma_f32_32x32x16_bf16 v[144:159], v[164:167], v[188:191], v[144:159]\n"
      "s_add_u32 s4, s4, 1024\n"
      "s_addc_u32 s5, s5, 0\n"
      "s_sub_u32 s6, s6, 1\n"
      "s_cmp_lg_u32 s6, 0\n"
      "s_cbranch_scc1 .Lq_Ln1_1\n"
      "s_nop 15\n"
      "s_branch .Lq_Ln1_ret\n"
      ".Lq_Lcode_end:\n"
      :
      : "{v223}"(threadIdx.x), "{s56}"(blockIdx.x), "{s[58:59]}"(__builtin_amdgcn_kernarg_segment_ptr()),
        "{s60}"((u32)(size_t)(__attribute__((address_space(3))) void*)lds)
      : "memory", "vcc", "v0", "v255", "a0", "a255", "s0", "s99");
}

extern "C" void kernel_launch(void* const* d_in, const int* in_sizes, int n_in, void* d_out, int out_size, void* d_ws,
                              size_t ws_size, hipStream_t stream) {
  fused<<<256, 256, 0, stream>>>(d_in[0], d_in[1], d_in[2], d_in[4], d_in[5], d_in[6], d_in[7], d_in[8], (char*)d_ws + 0, d_out);
}
```
